```python
import torch

def _softmax(x, axis=-1):
    return torch.softmax(x, dim=axis)
def _gelu(x):
    return torch.nn.functional.gelu(x, approximate="tanh")
def _silu(x):
    return torch.nn.functional.silu(x)
def _sigmoid(x):
    return torch.sigmoid(x)
def _layernorm(x, axis=-1, eps=1e-5):
    m = x.mean(dim=axis, keepdim=True)
    v = x.var(dim=axis, keepdim=True, unbiased=False)
    return (x - m) / torch.sqrt(v + eps)
def _scatter_add(tgt, idx, src, axis=0):
    return tgt.clone().index_add_(axis, idx, src)
def _segment_sum(src, seg, n):
    out = torch.zeros((n,) + src.shape[1:], dtype=src.dtype)
    return out.index_add_(0, seg, src)
def _pad(x, pw):
    flat = tuple(v for pair in reversed(pw) for v in pair)
    return torch.nn.functional.pad(x, flat)

kernel_name = 'standalone__mha_block__prompt'

def setup_inputs():

    _w = (lambda a: (lambda t: t.to(torch.bfloat16) if t.is_floating_point() else t)(torch.as_tensor(a)))
    B, S, D, H = 1, 2048, 448, 16
    return {
        "x": _w((lambda *s: torch.randn(*s, dtype=torch.bfloat16) * 0.2)(B, S, D)),
        "wqkv": _w((lambda *s: torch.randn(*s, dtype=torch.bfloat16) * 0.2)(D, 3 * D)),
        "wo": _w((lambda *s: torch.randn(*s, dtype=torch.bfloat16) * 0.05)(D, D)),
    }

def reference(x, wqkv, wo):
    B, S, D = x.shape
    H = 16
    Dh = D // H
    qkv = (lambda x, *d: x.permute(*d))((x @ wqkv).reshape(B, S, 3, H, Dh), 2, 0, 3, 1, 4)
    q, k, v = qkv[0], qkv[1], qkv[2]
    scores = torch.einsum("bhqd,bhkd->bhqk", q, k) * (Dh ** -0.5)
    p = _softmax(scores, axis=-1)
    o = torch.einsum("bhqk,bhkd->bhqd", p, v)
    return (lambda x, *d: x.permute(*d))(o, 0, 2, 1, 3).reshape(B, S, D) @ wo
```

```cpp
#include <hip/hip_runtime.h>
#include <hip/hip_bf16.h>
#include <stdint.h>

typedef __bf16 bf16x8 __attribute__((ext_vector_type(8)));
typedef __bf16 bf16x4 __attribute__((ext_vector_type(4)));
typedef float f32x16 __attribute__((ext_vector_type(16)));
typedef float f32x8 __attribute__((ext_vector_type(8)));
typedef float f32x4 __attribute__((ext_vector_type(4)));
typedef unsigned u32x4 __attribute__((ext_vector_type(4)));
typedef unsigned u32x2 __attribute__((ext_vector_type(2)));
typedef short s16x4 __attribute__((ext_vector_type(4)));
typedef short s16x8 __attribute__((ext_vector_type(8)));
#define LAS __attribute__((address_space(3)))
#define DI __device__ __forceinline__
#define MFMA32(a, b, c) __builtin_amdgcn_mfma_f32_32x32x16_bf16((a), (b), (c), 0, 0, 0)
#define MFMA16(a, b, c) __builtin_amdgcn_mfma_f32_16x16x32_bf16((a), (b), (c), 0, 0, 0)

constexpr int SEQ = 2048, DM = 448, NH = 16, DH = 28, N3 = 1344;
constexpr float QSCALE = 0.18898223650461362f * 1.4426950408889634f;
constexpr size_t WS_KV = 0, WS_Q = 4u << 20, WS_O = 6u << 20, WS_ST = 8u << 20;
constexpr int LDS_WKV = 0, LDS_WQ = 57344, LDS1 = 86016;
constexpr float SAFE_T2 = 60.f * 60.f;
constexpr float SAFE_V = 1.0e12f;

DI bf16x8 tr_frag(LAS const char* p0, LAS const char* p1) {
    s16x4 a = __builtin_bit_cast(s16x4, __builtin_amdgcn_ds_read_tr16_b64_v4i16((LAS s16x4*)p0));
    s16x4 b = __builtin_bit_cast(s16x4, __builtin_amdgcn_ds_read_tr16_b64_v4i16((LAS s16x4*)p1));
    return __builtin_bit_cast(bf16x8, __builtin_shufflevector(a, b, 0, 1, 2, 3, 4, 5, 6, 7));
}
DI bf16x8 pack_lo(const f32x16& c) { return __builtin_convertvector(__builtin_shufflevector(c, c, 0, 1, 2, 3, 4, 5, 6, 7), bf16x8); }
DI bf16x8 pack_hi(const f32x16& c) { return __builtin_convertvector(__builtin_shufflevector(c, c, 8, 9, 10, 11, 12, 13, 14, 15), bf16x8); }
DI float swap32(float v) {
    auto r = __builtin_amdgcn_permlane32_swap(__float_as_uint(v), __float_as_uint(v), false, false);
    return (threadIdx.x & 32) ? __uint_as_float(r[0]) : __uint_as_float(r[1]);
}
DI float wave_max(float v) {
#pragma unroll
    for (int o = 1; o < 64; o <<= 1) v = fmaxf(v, __shfl_xor(v, o));
    return v;
}
DI float nan_to_inf(float v) { return (v == v) ? v : INFINITY; }

__global__ __launch_bounds__(256, 2) void k_qkv(const __bf16* __restrict__ x, const __bf16* __restrict__ w, char* __restrict__ ws) {
    extern __shared__ __attribute__((aligned(16))) char lds_[];
    LAS char* lds = (LAS char*)lds_;
    const int bid = blockIdx.x, h = (bid & 7) * 2 + ((bid >> 3) & 1), r = bid >> 4;
    const int lane = threadIdx.x & 63, wid = __builtin_amdgcn_readfirstlane(threadIdx.x >> 6);
    const int c = lane & 31, hh = lane >> 5;
    const int T = 4 * r + wid;
    __amdgpu_buffer_rsrc_t rw = __builtin_amdgcn_make_buffer_rsrc((void*)w, 0, DM * N3 * 2, 0x00020000);
    bf16x8 xf[28];
    const __bf16* xp = x + (size_t)(32 * T + c) * DM + 8 * hh;
#pragma unroll
    for (int s = 0; s < 28; ++s) xf[s] = *(const bf16x8*)(xp + 16 * s);
    {
        const int seg = ((lane >> 2) & 1) ^ ((lane >> 4) & 1);
        const int vo = ((lane >> 3) * N3 + DM * (1 + seg) + h * DH + (lane & 3) * 8) * 2;
#pragma unroll
        for (int i = 0; i < 14; ++i) { const int inst = wid + 4 * i;
            __builtin_amdgcn_raw_ptr_buffer_load_lds(rw, (LAS void*)(lds + LDS_WKV + inst * 1024), 16, vo, inst * 8 * N3 * 2, 0, 0); }
        const int vq = ((lane >> 2) * N3 + h * DH + (lane & 3) * 8) * 2;
#pragma unroll
        for (int i = 0; i < 7; ++i) { const int inst = wid + 4 * i;
            __builtin_amdgcn_raw_ptr_buffer_load_lds(rw, (LAS void*)(lds + LDS_WQ + inst * 1024), 16, vq, inst * 16 * N3 * 2, 0, 0); }
    }
    __syncthreads();
    const int blk = (lane >> 4) & 1, q4 = (lane & 15) >> 2, p4 = lane & 3, sw = (q4 >> 1) & 1;
    LAS const char* kb = lds + LDS_WKV + (8 * hh + q4) * 128 + 64 * sw + 32 * blk + 8 * p4;
    LAS const char* vb = lds + LDS_WKV + (8 * hh + q4) * 128 + 64 * (1 ^ sw) + 32 * blk + 8 * p4;
    LAS const char* qb = lds + LDS_WQ + (8 * hh + q4) * 64 + 32 * blk + 8 * p4;
    f32x16 ak = {}, av = {}, aq = {};
#pragma unroll
    for (int s = 0; s < 28; ++s) {
        ak = MFMA32(tr_frag(kb + 2048 * s, kb + 2048 * s + 512), xf[s], ak);
        av = MFMA32(xf[s], tr_frag(vb + 2048 * s, vb + 2048 * s + 512), av);
        aq = MFMA32(tr_frag(qb + 1024 * s, qb + 1024 * s + 256), xf[s], aq);
    }
#pragma unroll
    for (int i = 0; i < 16; ++i) aq[i] *= QSCALE;
#pragma unroll
    for (int i = 12; i < 16; ++i) { ak[i] = hh ? 0.f : ak[i]; aq[i] = hh ? 0.f : aq[i]; }
    float kn = 0.f, qn = 0.f, vm = 0.f;
#pragma unroll
    for (int i = 0; i < 16; ++i) { kn = fmaf(ak[i], ak[i], kn); qn = fmaf(aq[i], aq[i], qn); av[i] = (c < DH) ? av[i] : (c == DH ? 1.f : 0.f); vm += fabsf(av[i]); }
    kn += swap32(kn); qn += swap32(qn);
    kn = wave_max(nan_to_inf(kn)); qn = wave_max(nan_to_inf(qn)); vm = wave_max(nan_to_inf(vm));
    char* kv = ws + WS_KV + (size_t)(h * 64 + T) * 4096 + lane * 16;
    *(bf16x8*)(kv) = pack_lo(ak); *(bf16x8*)(kv + 1024) = pack_hi(ak);
    *(bf16x8*)(kv + 2048) = pack_lo(av); *(bf16x8*)(kv + 3072) = pack_hi(av);
    char* qo = ws + WS_Q + (size_t)(h * 64 + T) * 2048 + lane * 16;
    *(bf16x8*)(qo) = pack_lo(aq); *(bf16x8*)(qo + 1024) = pack_hi(aq);
    if (lane == 0) *(f32x4*)(ws + WS_ST + (size_t)(h * 64 + T) * 16) = (f32x4){kn, vm, qn, 0.f};
}

__global__ __launch_bounds__(256, 2) void k_attn(char* __restrict__ ws) {
    extern __shared__ __attribute__((aligned(16))) char lds_[];
    LAS char* lds = (LAS char*)lds_;
    const int bid = blockIdx.x, h = (bid & 7) * 2 + ((bid >> 3) & 1), r = bid >> 4;
    const int lane = threadIdx.x & 63, wid = __builtin_amdgcn_readfirstlane(threadIdx.x >> 6);
    const int c = lane & 31, hh = lane >> 5;
    bf16x8 qf[4][2];
    const char* qp = ws + WS_Q + (size_t)(h * 64 + 4 * r) * 2048 + lane * 16;
#pragma unroll
    for (int t = 0; t < 4; ++t) { qf[t][0] = *(const bf16x8*)(qp + t * 2048); qf[t][1] = *(const bf16x8*)(qp + t * 2048 + 1024); }
    const char* kvp = ws + WS_KV + (size_t)(h * 64 + 16 * wid) * 4096 + lane * 16;
    const f32x4 stk = *(const f32x4*)(ws + WS_ST + (size_t)(h * 64 + 16 * wid + (lane & 15)) * 16);
    const f32x4 stq = *(const f32x4*)(ws + WS_ST + (size_t)(h * 64 + 4 * r + (lane & 3)) * 16);
    const float kn = wave_max(stk[0]), vm = wave_max(stk[1]), qn = wave_max(stq[2]);
    const bool fast = __builtin_amdgcn_readfirstlane((kn * qn * 1.05f <= SAFE_T2 && vm <= SAFE_V) ? 1 : 0) != 0;
    f32x16 o[4] = {};
    float m[4] = {0.f, 0.f, 0.f, 0.f};
    if (fast) {
        bf16x8 k0 = *(const bf16x8*)(kvp), k1 = *(const bf16x8*)(kvp + 1024), v0 = *(const bf16x8*)(kvp + 2048), v1 = *(const bf16x8*)(kvp + 3072);
        bf16x8 nk0 = *(const bf16x8*)(kvp + 4096), nk1 = *(const bf16x8*)(kvp + 4096 + 1024), nv0 = *(const bf16x8*)(kvp + 4096 + 2048), nv1 = *(const bf16x8*)(kvp + 4096 + 3072);
#pragma unroll 2
        for (int j = 0; j < 16; ++j) {
            const int jn = j + 2 < 16 ? j + 2 : 15;
            const char* np = kvp + jn * 4096;
            bf16x8 mk0 = *(const bf16x8*)(np), mk1 = *(const bf16x8*)(np + 1024), mv0 = *(const bf16x8*)(np + 2048), mv1 = *(const bf16x8*)(np + 3072);
#pragma unroll
            for (int t = 0; t < 4; ++t) {
                f32x16 s = {};
                s = MFMA32(k0, qf[t][0], s);
                s = MFMA32(k1, qf[t][1], s);
#pragma unroll
                for (int i = 0; i < 16; ++i) s[i] = __builtin_amdgcn_exp2f(s[i]);
                o[t] = MFMA32(v0, pack_lo(s), o[t]);
                o[t] = MFMA32(v1, pack_hi(s), o[t]);
            }
            k0 = nk0; k1 = nk1; v0 = nv0; v1 = nv1;
            nk0 = mk0; nk1 = mk1; nv0 = mv0; nv1 = mv1;
        }
    } else {
#pragma unroll
        for (int t = 0; t < 4; ++t) m[t] = -INFINITY;
        for (int j = 0; j < 16; ++j) {
            const char* np = kvp + j * 4096;
            const bf16x8 k0 = *(const bf16x8*)(np), k1 = *(const bf16x8*)(np + 1024), v0 = *(const bf16x8*)(np + 2048), v1 = *(const bf16x8*)(np + 3072);
#pragma unroll
            for (int t = 0; t < 4; ++t) {
                f32x16 s = {};
                s = MFMA32(k0, qf[t][0], s);
                s = MFMA32(k1, qf[t][1], s);
                float bm = s[0];
#pragma unroll
                for (int i = 1; i < 16; ++i) bm = fmaxf(bm, s[i]);
                bm = fmaxf(bm, swap32(bm));
                const float mn = fmaxf(m[t], bm), al = __builtin_amdgcn_exp2f(m[t] - mn);
                m[t] = mn;
#pragma unroll
                for (int i = 0; i < 16; ++i) { o[t][i] *= al; s[i] = __builtin_amdgcn_exp2f(s[i] - mn); }
                o[t] = MFMA32(v0, pack_lo(s), o[t]);
                o[t] = MFMA32(v1, pack_hi(s), o[t]);
            }
        }
    }
    LAS float* mt = (LAS float*)(lds + 65536);
#pragma unroll
    for (int t = 0; t < 4; ++t) {
        mt[(t * 4 + wid) * 64 + lane] = m[t];
        if (t != wid) {
#pragma unroll
            for (int g = 0; g < 4; ++g) *(LAS f32x4*)(lds + ((t * 4 + wid) * 4 + g) * 1024 + lane * 16) = (f32x4){o[t][4 * g], o[t][4 * g + 1], o[t][4 * g + 2], o[t][4 * g + 3]};
        }
    }
    f32x16 acc = wid == 0 ? o[0] : wid == 1 ? o[1] : wid == 2 ? o[2] : o[3];
    __syncthreads();
    float ms[4], M = -INFINITY;
#pragma unroll
    for (int s = 0; s < 4; ++s) { ms[s] = mt[(wid * 4 + s) * 64 + lane]; M = fmaxf(M, ms[s]); }
    const bool plain = __all(ms[0] == 0.f && ms[1] == 0.f && ms[2] == 0.f && ms[3] == 0.f);
    if (plain) {
#pragma unroll
        for (int d = 1; d < 4; ++d) { const int s = (wid + d) & 3;
#pragma unroll
            for (int g = 0; g < 4; ++g) { const f32x4 p = *(LAS const f32x4*)(lds + ((wid * 4 + s) * 4 + g) * 1024 + lane * 16);
#pragma unroll
                for (int i = 0; i < 4; ++i) acc[4 * g + i] += p[i]; } }
    } else {
        const float mo = mt[(wid * 4 + wid) * 64 + lane], f0 = __builtin_amdgcn_exp2f(mo - M);
#pragma unroll
        for (int i = 0; i < 16; ++i) acc[i] *= f0;
#pragma unroll
        for (int d = 1; d < 4; ++d) { const int s = (wid + d) & 3; const float f = __builtin_amdgcn_exp2f(mt[(wid * 4 + s) * 64 + lane] - M);
#pragma unroll
            for (int g = 0; g < 4; ++g) { const f32x4 p = *(LAS const f32x4*)(lds + ((wid * 4 + s) * 4 + g) * 1024 + lane * 16);
#pragma unroll
                for (int i = 0; i < 4; ++i) acc[4 * g + i] = fmaf(p[i], f, acc[4 * g + i]); } }
    }
    const float lsw = swap32(acc[12]);
    const float inv = 1.0f / (hh ? acc[12] : lsw);
#pragma unroll
    for (int i = 0; i < 16; ++i) acc[i] *= inv;
#pragma unroll
    for (int i = 12; i < 16; ++i) acc[i] = hh ? 0.f : acc[i];
    char* op = ws + WS_O + (size_t)((8 * r + 2 * wid + (c >> 4)) * 16 + h) * 1024 + hh * 256 + (c & 15) * 16;
    *(bf16x8*)(op) = pack_lo(acc);
    *(bf16x8*)(op + 512) = pack_hi(acc);
}

__global__ __launch_bounds__(256, 2) void k_proj(const __bf16* __restrict__ wo, const char* __restrict__ ws, __bf16* __restrict__ out) {
    extern __shared__ __attribute__((aligned(16))) char lds_[];
    LAS char* lds = (LAS char*)lds_;
    const int bid = blockIdx.x, rb = bid >> 2, cq = bid & 3;
    const int lane = threadIdx.x & 63, wid = __builtin_amdgcn_readfirstlane(threadIdx.x >> 6);
    const int rt = wid & 1, kh = wid >> 1;
    bf16x8 of[8];
    const char* op = ws + WS_O + (size_t)((2 * rb + rt) * 16 + 8 * kh) * 1024 + lane * 16;
#pragma unroll
    for (int i = 0; i < 8; ++i) of[i] = *(const bf16x8*)(op + i * 1024);
    for (int inst = wid; inst < 98; inst += 4) { const int i = inst * 64 + lane, row = i / 14, cc = i - row * 14;
        __builtin_amdgcn_global_load_lds((const void __attribute__((address_space(1)))*)(wo + row * DM + cq * 112 + cc * 8), (LAS void*)(lds + inst * 1024), 16, 0, 0); }
    __syncthreads();
    const int g = lane >> 4, q4 = (lane & 15) >> 2, p4 = lane & 3, row0 = 16 * (g >> 1) + 4 * (g & 1) + q4;
    LAS const char* b1 = lds + (kh * 8 * DH + row0) * 224 + 8 * p4;
    LAS const char* b2 = lds + (kh * 8 * DH + row0 + (g == 3 ? 4 : 8)) * 224 + 8 * p4;
    f32x4 acc[7] = {};
#pragma unroll
    for (int i = 0; i < 8; ++i)
#pragma unroll
        for (int ct = 0; ct < 7; ++ct) acc[ct] = MFMA16(tr_frag(b1 + i * 6272 + ct * 32, b2 + i * 6272 + ct * 32), of[i], acc[ct]);
    LAS char* ex = lds + 100352 + rt * 7168;
#pragma unroll
    for (int ct = 0; ct < 7; ++ct) if ((ct < 4) == (kh == 1)) *(LAS f32x4*)(ex + ct * 1024 + lane * 16) = acc[ct];
    __syncthreads();
    __bf16* orow = out + (size_t)(32 * rb + 16 * rt + (lane & 15)) * DM + cq * 112 + 4 * g;
#pragma unroll
    for (int ct = 0; ct < 7; ++ct) if ((ct < 4) == (kh == 0)) {
        const f32x4 v = acc[ct] + *(LAS const f32x4*)(ex + ct * 1024 + lane * 16);
        *(bf16x4*)(orow + 16 * ct) = __builtin_convertvector(v, bf16x4);
    }
}

extern "C" void kernel_launch(void* const* d_in, const int* in_sizes, int n_in, void* d_out, int out_size, void* d_ws, size_t ws_size, hipStream_t stream) {
    const __bf16* x = (const __bf16*)d_in[0];
    const __bf16* wqkv = (const __bf16*)d_in[1];
    const __bf16* wo = (const __bf16*)d_in[2];
    char* ws = (char*)d_ws;
    (void)hipFuncSetAttribute((const void*)k_qkv, hipFuncAttributeMaxDynamicSharedMemorySize, LDS1);
    (void)hipFuncSetAttribute((const void*)k_attn, hipFuncAttributeMaxDynamicSharedMemorySize, 65536 + 4096);
    (void)hipFuncSetAttribute((const void*)k_proj, hipFuncAttributeMaxDynamicSharedMemorySize, 100352 + 14336);
    k_qkv<<<256, 256, LDS1, stream>>>(x, wqkv, ws);
    k_attn<<<256, 256, 65536 + 4096, stream>>>(ws);
    k_proj<<<256, 256, 100352 + 14336, stream>>>(wo, ws, (__bf16*)d_out);
}
```

```cpp
#include <hip/hip_runtime.h>
#include <hip/hip_bf16.h>
#include <stdint.h>

typedef __bf16 bf16x8 __attribute__((ext_vector_type(8)));
typedef __bf16 bf16x4 __attribute__((ext_vector_type(4)));
typedef __bf16 bf16x2 __attribute__((ext_vector_type(2)));
typedef float f32x16 __attribute__((ext_vector_type(16)));
typedef float f32x8 __attribute__((ext_vector_type(8)));
typedef float f32x4 __attribute__((ext_vector_type(4)));
typedef float f32x2 __attribute__((ext_vector_type(2)));
typedef unsigned u32x4 __attribute__((ext_vector_type(4)));
typedef unsigned u32x2 __attribute__((ext_vector_type(2)));
typedef short s16x4 __attribute__((ext_vector_type(4)));
typedef unsigned long long u64;
#define LAS __attribute__((address_space(3)))
#define GAS __attribute__((address_space(1)))
#define DI __device__ __forceinline__
#define MFMA32(a, b, c) __builtin_amdgcn_mfma_f32_32x32x16_bf16((a), (b), (c), 0, 0, 0)
#define MFMA16(a, b, c) __builtin_amdgcn_mfma_f32_16x16x32_bf16((a), (b), (c), 0, 0, 0)
#define SB() __builtin_amdgcn_sched_barrier(0)
#define PIN(x) asm volatile("" : "+v"(x))
#define RLX_AGENT __ATOMIC_RELAXED, __HIP_MEMORY_SCOPE_AGENT
#define RLX_WG __ATOMIC_RELAXED, __HIP_MEMORY_SCOPE_WORKGROUP
#define SC1 16
#define BF(x) __builtin_bit_cast(bf16x8, x)
#define U4(x) __builtin_bit_cast(u32x4, x)
#define MAPV 2
#define ROTV 1

constexpr int SEQ = 2048, DM = 448, NH = 16, DH = 28, N3 = 1344;
constexpr float QSCALE = 0.18898223650461362f * 1.4426950408889634f;
constexpr unsigned WS_KV = 0, WS_O = 4u << 20, WS_FLAG = 6u << 20, WS_EPOCH = WS_FLAG + 65536, WS_END = WS_EPOCH + 256;
constexpr int SLOT = 24576, LDS_WQ = SLOT, LDS_WO = 0, LDS_CMB = 100352, LDS_Q = 149504, LDS_MISC = 157696, LDS_BYTES = LDS_MISC + 64;
constexpr float SAFE_T2 = 90.f * 90.f;
constexpr float SAFE_V = 1.0e6f;
constexpr unsigned SPIN_CAP = 1u << 18;
constexpr unsigned SENT = 0xFFFFFFFFu;

#define STAMP(i)
#define STAMPC(i)

DI bf16x8 tr_frag(LAS const char* p0, LAS const char* p1) {
    s16x4 a = __builtin_bit_cast(s16x4, __builtin_amdgcn_ds_read_tr16_b64_v4i16((LAS s16x4*)p0));
    s16x4 b = __builtin_bit_cast(s16x4, __builtin_amdgcn_ds_read_tr16_b64_v4i16((LAS s16x4*)p1));
    return __builtin_bit_cast(bf16x8, __builtin_shufflevector(a, b, 0, 1, 2, 3, 4, 5, 6, 7));
}
DI u32x4 pack_lo(const f32x16& c) { return U4(__builtin_convertvector(__builtin_shufflevector(c, c, 0, 1, 2, 3, 4, 5, 6, 7), bf16x8)); }
DI u32x4 pack_hi(const f32x16& c) { return U4(__builtin_convertvector(__builtin_shufflevector(c, c, 8, 9, 10, 11, 12, 13, 14, 15), bf16x8)); }
DI unsigned cvt2(float a, float b) { return __builtin_bit_cast(unsigned, __builtin_convertvector((f32x2){a, b}, bf16x2)); }
DI float swap32(float v, bool hi) {
    auto r = __builtin_amdgcn_permlane32_swap(__float_as_uint(v), __float_as_uint(v), false, false);
    return hi ? __uint_as_float(r[0]) : __uint_as_float(r[1]);
}
DI unsigned pos_bits(float v) { return (v == v) ? __float_as_uint(v) : 0x7F800000u; }
DI void dma16(u32x4 rsrc, unsigned voff, unsigned soff, unsigned lds_dst) {
    unsigned keep;
    asm volatile("s_mov_b32 %0, m0\n\ts_mov_b32 m0, %4\n\ts_nop 0\n\tbuffer_load_dwordx4 %1, %2, %3 offen lds\n\ts_mov_b32 m0, %0"
                 : "=&s"(keep) : "v"(voff), "s"(rsrc), "s"(soff), "s"(lds_dst) : "memory");
}
DI u32x4 mk_rsrc(const void* p, unsigned bytes) { const u64 a = (u64)p; return (u32x4){(unsigned)a, (unsigned)(a >> 32) & 0xffffu, bytes, 0x00020000u}; }
#define WAITV(n) asm volatile("s_waitcnt vmcnt(" #n ")" ::: "memory")
#define BAR() asm volatile("s_waitcnt lgkmcnt(0)\n\ts_barrier" ::: "memory")
DI void lds_max(unsigned addr, unsigned v) { asm volatile("ds_max_u32 %0, %1" :: "v"(addr), "v"(v) : "memory"); }
DI void sync4(LAS unsigned* cnt, int lane) {
    asm volatile("s_waitcnt lgkmcnt(0)" ::: "memory");
    if (lane == 0) __hip_atomic_fetch_add(cnt, 1u, RLX_WG);
    while (__builtin_amdgcn_readfirstlane(__hip_atomic_load(cnt, RLX_WG)) < 4u) __builtin_amdgcn_s_sleep(1);
    asm volatile("" ::: "memory");
}

__global__ __launch_bounds__(512, 2) void k_mha(const __bf16* __restrict__ x, const __bf16* __restrict__ w, const __bf16* __restrict__ wo, char* ws, __bf16* __restrict__ out) {
    extern __shared__ __attribute__((aligned(16))) char lds_[];
    LAS char* lds = (LAS char*)lds_;
    LAS unsigned* misc = (LAS unsigned*)(lds + LDS_MISC);
    const unsigned lds0 = (unsigned)(uintptr_t)lds;
    const int bid = blockIdx.x;
    const int h = MAPV == 0 ? (bid & 7) * 2 + ((bid >> 3) & 1) : MAPV == 2 ? ((bid & 1) * 8 + ((bid >> 3) & 7)) : ((bid & 3) * 4 + ((bid >> 3) & 3));
    const int r = MAPV == 0 ? bid >> 4 : MAPV == 2 ? (((bid >> 1) & 3) * 4 + (bid >> 6)) : (((bid >> 2) & 1) * 8 + (bid >> 5));
    const int lane = threadIdx.x & 63, wid = __builtin_amdgcn_readfirstlane(threadIdx.x >> 6), w4 = wid & 3;
    const int c = lane & 31; const bool hh = lane >= 32;
    const int T = 4 * r + w4;
    const int rot = ROTV ? (r + h) % 7 : 0;
    STAMP(0);
    const __amdgpu_buffer_rsrc_t rws = __builtin_amdgcn_make_buffer_rsrc(ws, 0, WS_END, 0x00020000);
    const int blk = (lane >> 4) & 1, q4 = (lane & 15) >> 2, p4 = lane & 3, h8 = hh ? 8 : 0;
    const unsigned xa0 = w4 * 4096 + c * 128 + (((hh ? 1 : 0) ^ ((c >> 1) & 7)) * 16);

    if (wid < 4) {
        const u64 epoch = __hip_atomic_load((GAS u64*)(ws + WS_EPOCH), RLX_AGENT);
        if (threadIdx.x < 5) misc[threadIdx.x] = 0u;
        const unsigned ooff = WS_O + ((8 * r + 2 * w4 + (c >> 4)) * 16 + h) * 1024 + (hh ? 256 : 0) + (c & 15) * 16;
        __builtin_amdgcn_raw_buffer_store_b128((u32x4){SENT, SENT, SENT, SENT}, rws, ooff, 0, SC1);
        __builtin_amdgcn_raw_buffer_store_b128((u32x4){SENT, SENT, SENT, SENT}, rws, ooff + 512, 0, SC1);
        const bool isk = h < 7, isv = h >= 7 && h < 14;
        const int ct = isk ? h : h - 7;
        unsigned dst[8];
        if (isk) {
#pragma unroll
            for (int i = 0; i < 8; ++i) { const int cg = 16 * ct + 2 * i + (hh ? 1 : 0), hd = (cg * 147) >> 10, dg = cg - 7 * hd;
                dst[i] = WS_KV + (hd * 64 + T) * 4096 + (dg == 5 ? 1536 + 8 * c : (dg >> 2) * 1024 + (dg & 1) * 512 + ((dg >> 1) & 1) * 8 + 16 * c); }
        } else {
#pragma unroll
            for (int a = 0; a < 2; ++a) { const int cc = 64 * ct + 32 * a + c, hd = (cc * 293) >> 13, d = cc - DH * hd;
                dst[a] = WS_KV + (hd * 64 + T) * 4096 + 1792 + (hh ? 448 : 0) + 16 * d;
                dst[2 + a] = (d == 0 && !hh) ? WS_KV + (hd * 64 + T) * 4096 + 3584 : 0xFFFFFF00u; }
#pragma unroll
            for (int i = 4; i < 8; ++i) dst[i] = 0;
        }
        const u64 tag = (((u64)__builtin_amdgcn_readfirstlane((unsigned)(epoch >> 32)) << 32 | (unsigned)__builtin_amdgcn_readfirstlane((unsigned)epoch)) + 1ull) & 0xFFFFFFFFFFFFull;
        if (threadIdx.x == 0) { misc[5] = (unsigned)tag; misc[6] = (unsigned)(tag >> 32); }
        const int sw = (q4 >> 1) & 1;
        const unsigned wa0 = 16384 + (h8 + q4) * 128 + 64 * sw + 32 * blk + 8 * p4, wa1 = wa0 ^ 64;
        f32x16 a0 = {}, a1 = {};
        STAMP(1);
#define P1CHUNK(p, KT) do { BAR(); \
            LAS const char* sl = lds + ((p) % 6) * SLOT; bf16x8 xf_[4], w0_[4], w1_[4]; \
            _Pragma("unroll") for (int s = 0; s < 4; ++s) { xf_[s] = *(LAS const bf16x8*)(sl + (xa0 ^ (32 * s))); \
                w0_[s] = tr_frag(sl + wa0 + 2048 * s, sl + wa0 + 2048 * s + 512); w1_[s] = tr_frag(sl + wa1 + 2048 * s, sl + wa1 + 2048 * s + 512); } \
            SB(); \
            _Pragma("unroll") for (int s = 0; s < 4; ++s) { \
                if (KT) { a0 = MFMA32(w0_[s], xf_[s], a0); a1 = MFMA32(w1_[s], xf_[s], a1); } else { a0 = MFMA32(xf_[s], w0_[s], a0); a1 = MFMA32(xf_[s], w1_[s], a1); } } \
            SB(); } while (0)
        float stat = 0.f;
        if (isk) {
            P1CHUNK(0, 1); STAMP(2); P1CHUNK(1, 1); P1CHUNK(2, 1); P1CHUNK(3, 1); STAMP(3); P1CHUNK(4, 1); P1CHUNK(5, 1); P1CHUNK(6, 1);
            STAMP(4);
            const u32x4 l0 = pack_lo(a0), h0 = pack_hi(a0), l1 = pack_lo(a1), h1 = pack_hi(a1);
            __builtin_amdgcn_raw_buffer_store_b64((u32x2){l0[0], l0[1]}, rws, dst[0], 0, SC1); __builtin_amdgcn_raw_buffer_store_b64((u32x2){l0[2], l0[3]}, rws, dst[1], 0, SC1);
            __builtin_amdgcn_raw_buffer_store_b64((u32x2){h0[0], h0[1]}, rws, dst[2], 0, SC1); __builtin_amdgcn_raw_buffer_store_b64((u32x2){h0[2], h0[3]}, rws, dst[3], 0, SC1);
            __builtin_amdgcn_raw_buffer_store_b64((u32x2){l1[0], l1[1]}, rws, dst[4], 0, SC1); __builtin_amdgcn_raw_buffer_store_b64((u32x2){l1[2], l1[3]}, rws, dst[5], 0, SC1);
            __builtin_amdgcn_raw_buffer_store_b64((u32x2){h1[0], h1[1]}, rws, dst[6], 0, SC1); __builtin_amdgcn_raw_buffer_store_b64((u32x2){h1[2], h1[3]}, rws, dst[7], 0, SC1);
#pragma unroll
            for (int i = 0; i < 16; ++i) { stat = fmaf(a0[i], a0[i], stat); stat = fmaf(a1[i], a1[i], stat); }
            stat += swap32(stat, hh);
        } else if (isv) {
            P1CHUNK(0, 0); STAMP(2); P1CHUNK(1, 0); P1CHUNK(2, 0); P1CHUNK(3, 0); STAMP(3); P1CHUNK(4, 0); P1CHUNK(5, 0); P1CHUNK(6, 0);
            STAMP(4);
            __builtin_amdgcn_raw_buffer_store_b128(pack_lo(a0), rws, dst[0], 0, SC1); __builtin_amdgcn_raw_buffer_store_b128(pack_hi(a0), rws, dst[0] + 896, 0, SC1);
            __builtin_amdgcn_raw_buffer_store_b128(pack_lo(a1), rws, dst[1], 0, SC1); __builtin_amdgcn_raw_buffer_store_b128(pack_hi(a1), rws, dst[1] + 896, 0, SC1);
            const u32x4 ones = {0x3F803F80u, 0x3F803F80u, 0x3F803F80u, 0x3F803F80u};
            __builtin_amdgcn_raw_buffer_store_b128(ones, rws, dst[2], 0, SC1); __builtin_amdgcn_raw_buffer_store_b128(ones, rws, dst[3], 0, SC1);
#pragma unroll
            for (int i = 0; i < 16; ++i) stat += fabsf(a0[i]) + fabsf(a1[i]);
        } else {
            BAR(); STAMP(2); BAR(); BAR(); BAR(); STAMP(3); BAR(); BAR(); BAR(); STAMP(4);
        }
        lds_max(lds0 + LDS_MISC + 4, pos_bits(stat));
        STAMP(5);
        asm volatile("s_waitcnt vmcnt(0) lgkmcnt(0)" ::: "memory");
        if (lane == 0) {
            if (__hip_atomic_fetch_add(misc, 1u, RLX_WG) == 3u) {
                const unsigned sb = (__hip_atomic_load(misc + 1, RLX_WG) + 0xFFFFu) >> 16;
                __hip_atomic_store((GAS u64*)(ws + WS_FLAG + (r * 16 + h) * 256), tag << 16 | sb, RLX_AGENT);
            }
        }
        STAMP(6);
        const int ca = (DH * h) >> 6, cb = (DH * h + DH - 1) >> 6;
        GAS u64* fp = (GAS u64*)(ws + WS_FLAG + ((4 * w4 + ((lane >> 2) & 3)) * 16 + ((lane & 1) ? cb : ca) + ((lane & 2) ? 7 : 0)) * 256);
        u64 fv;
        for (unsigned spins = 0;; ++spins) {
            fv = __hip_atomic_load(fp, RLX_AGENT);
            if (__all((fv >> 16) == tag) || spins > SPIN_CAP) break;
            __builtin_amdgcn_s_sleep(1);
        }
        asm volatile("" ::: "memory");
        STAMP(7);
        const unsigned kb0 = 16 * lane, kb1 = hh ? 1536 + 8 * c : 1024 + 16 * c, vb0 = c < DH ? 1792 + (hh ? 448 : 0) + 16 * c : 3584, vb1 = c < DH ? vb0 + 896 : 3584;
        const unsigned sbase = WS_KV + (h * 64 + 16 * w4) * 4096;
#define LDKV(rs_, vo_, so_) __builtin_amdgcn_raw_buffer_load_b128(rs_, vo_, so_, SC1)
        u32x4 kf[4][2], vf[4][2];
#pragma unroll
        for (int b = 0; b < 4; ++b) { kf[b][0] = LDKV(rws, kb0, sbase + b * 4096); SB(); kf[b][1] = LDKV(rws, kb1, sbase + b * 4096); SB();
            if (b < 3) { vf[b][0] = LDKV(rws, vb0, sbase + b * 4096); SB(); vf[b][1] = LDKV(rws, vb1, sbase + b * 4096); SB(); } }
        vf[3][0] = (u32x4){0u, 0u, 0u, 0u}; vf[3][1] = vf[3][0];
        SB();
        unsigned sa_ = 0, sb_ = 0, sv_ = 0;
        { const unsigned s16 = (unsigned)fv << 16;
#pragma unroll
          for (int i = 0; i < 4; ++i) { sa_ = max(sa_, (unsigned)__builtin_amdgcn_readlane(s16, 4 * i)); sb_ = max(sb_, (unsigned)__builtin_amdgcn_readlane(s16, 4 * i + 1));
              sv_ = max(sv_, max((unsigned)__builtin_amdgcn_readlane(s16, 4 * i + 2), (unsigned)__builtin_amdgcn_readlane(s16, 4 * i + 3))); } }
        const float kn = __uint_as_float(sa_) + (ca != cb ? __uint_as_float(sb_) : 0.f);
        BAR();
        STAMP(8);
        bf16x8 qf[4][2];
#pragma unroll
        for (int t = 0; t < 4; ++t) { qf[t][0] = *(LAS const bf16x8*)(lds + LDS_Q + t * 2048 + lane * 16); qf[t][1] = *(LAS const bf16x8*)(lds + LDS_Q + t * 2048 + 1024 + lane * 16); }
        const float qn = __uint_as_float(misc[2]);
        const bool fast = __builtin_amdgcn_readfirstlane((kn * qn * 1.05f <= SAFE_T2 && __uint_as_float(sv_) <= SAFE_V) ? 1 : 0) != 0;
        f32x16 o[4] = {};
        float m[4] = {0.f, 0.f, 0.f, 0.f};
        STAMP(9); STAMPC(14);
        if (fast) {
            f32x16 sa, sb = {};
            u32x4 plo = {}, phi = {};
            const f32x16 zero = {};
            sa = MFMA32(BF(kf[0][0]), qf[0][0], zero); sa = MFMA32(BF(kf[0][1]), qf[0][1], sa);
            SB();
#define EX(S, i) S[i] = __builtin_amdgcn_exp2f(S[i])
#define STEP(SC, SN, B, t, LD) do { \
            constexpr int tn = ((t) + 1) & 3, bn = (t) == 3 ? ((B) + 1) & 3 : (B), tp = ((t) + 3) & 3, bp = (t) == 0 ? ((B) + 3) & 3 : (B); \
            o[tp] = MFMA32(BF(vf[bp][0]), BF(plo), o[tp]); SB(); \
            phi[2] = cvt2(SN[12], SN[13]); phi[3] = cvt2(SN[14], SN[15]); PIN(phi); SB(); EX(SC, 0); EX(SC, 1); EX(SC, 2); EX(SC, 3); PIN(SC); SB(); \
            SN = MFMA32(BF(kf[bn][0]), qf[tn][0], zero); SB(); \
            plo[0] = cvt2(SC[0], SC[1]); plo[1] = cvt2(SC[2], SC[3]); PIN(plo); SB(); EX(SC, 4); EX(SC, 5); EX(SC, 6); EX(SC, 7); PIN(SC); SB(); \
            SN = MFMA32(BF(kf[bn][1]), qf[tn][1], SN); SB(); \
            plo[2] = cvt2(SC[4], SC[5]); plo[3] = cvt2(SC[6], SC[7]); PIN(plo); SB(); EX(SC, 8); EX(SC, 9); EX(SC, 10); EX(SC, 11); PIN(SC); SB(); \
            o[tp] = MFMA32(BF(vf[bp][1]), BF(phi), o[tp]); SB(); \
            LD; SB(); \
            phi[0] = cvt2(SC[8], SC[9]); phi[1] = cvt2(SC[10], SC[11]); PIN(phi); SB(); EX(SC, 12); EX(SC, 13); EX(SC, 14); EX(SC, 15); PIN(SC); SB(); \
            } while (0)
#define PIN4(a, b, c_, d) asm volatile("" : "+v"(a), "+v"(b), "+v"(c_), "+v"(d))
#define LDV(B) do { if ((B) == 0) { vf[3][0] = LDKV(rws, vb0, so - 4096); vf[3][1] = LDKV(rws, vb1, so - 4096); } \
                    else { vf[(B) - 1][0] = LDKV(rn, vb0, so + ((B) - 1) * 4096); vf[(B) - 1][1] = LDKV(rn, vb1, so + ((B) - 1) * 4096); } \
                    PIN4(vf[B][0], vf[B][1], kf[((B) + 1) & 3][0], kf[((B) + 1) & 3][1]); } while (0)
#define LDK(B) do { kf[B][0] = LDKV(rn, kb0, so + (B) * 4096); kf[B][1] = LDKV(rn, kb1, so + (B) * 4096); } while (0)
#define BLOCK(B) STEP(sa, sb, B, 0, LDV(B)); STEP(sb, sa, B, 1, (void)0); STEP(sa, sb, B, 2, LDK(B)); STEP(sb, sa, B, 3, (void)0)
            for (int it = 0; it < 4; ++it) {
                const unsigned so = sbase + (unsigned)(it + 1) * 16384u;
                const __amdgpu_buffer_rsrc_t rn = __builtin_amdgcn_make_buffer_rsrc(ws, 0, it < 3 ? WS_END : 0u, 0x00020000);
                BLOCK(0); BLOCK(1); BLOCK(2); BLOCK(3);
            }
            o[3] = MFMA32(BF(vf[3][0]), BF(plo), o[3]);
            phi[2] = cvt2(sb[12], sb[13]); phi[3] = cvt2(sb[14], sb[15]);
            o[3] = MFMA32(BF(vf[3][1]), BF(phi), o[3]);
        } else {
#pragma unroll
            for (int t = 0; t < 4; ++t) m[t] = -INFINITY;
            for (int j = 0; j < 16; ++j) {
                const unsigned so = sbase + j * 4096;
                const bf16x8 k0 = BF(LDKV(rws, kb0, so)), k1 = BF(LDKV(rws, kb1, so)), v0 = BF(LDKV(rws, vb0, so)), v1 = BF(LDKV(rws, vb1, so));
#pragma unroll
                for (int t = 0; t < 4; ++t) {
                    f32x16 s = {};
                    s = MFMA32(k0, qf[t][0], s);
                    s = MFMA32(k1, qf[t][1], s);
                    float bm = s[0];
#pragma unroll
                    for (int i = 1; i < 16; ++i) bm = fmaxf(bm, s[i]);
                    bm = fmaxf(bm, swap32(bm, hh));
                    const float mn = fmaxf(m[t], bm), al = __builtin_amdgcn_exp2f(m[t] - mn);
                    m[t] = mn;
#pragma unroll
                    for (int i = 0; i < 16; ++i) { o[t][i] *= al; s[i] = __builtin_amdgcn_exp2f(s[i] - mn); }
                    o[t] = MFMA32(v0, BF(pack_lo(s)), o[t]);
                    o[t] = MFMA32(v1, BF(pack_hi(s)), o[t]);
                }
            }
        }
        STAMPC(15); STAMP(10);
#pragma unroll
        for (int t = 0; t < 4; ++t) {
            o[t][13] = hh ? m[t] : o[t][13];
            if (t != w4) {
                const int d = (w4 - t) & 3;
#pragma unroll
                for (int g = 0; g < 4; ++g) *(LAS f32x4*)(lds + LDS_CMB + ((t * 3 + d - 1) * 4 + g) * 1024 + lane * 16) = (f32x4){o[t][4 * g], o[t][4 * g + 1], o[t][4 * g + 2], o[t][4 * g + 3]};
            }
        }
        f32x16 acc = w4 == 0 ? o[0] : w4 == 1 ? o[1] : w4 == 2 ? o[2] : o[3];
        BAR();
        f32x16 p[3];
#pragma unroll
        for (int d = 0; d < 3; ++d)
#pragma unroll
            for (int g = 0; g < 4; ++g) { const f32x4 v = *(LAS const f32x4*)(lds + LDS_CMB + ((w4 * 3 + d) * 4 + g) * 1024 + lane * 16);
#pragma unroll
                for (int i = 0; i < 4; ++i) p[d][4 * g + i] = v[i]; }
        const bool plain = __all(!hh || (acc[13] == 0.f && p[0][13] == 0.f && p[1][13] == 0.f && p[2][13] == 0.f));
        if (plain) {
            acc = (acc + p[0]) + (p[1] + p[2]);
        } else {
            float mm[4] = {acc[13], p[0][13], p[1][13], p[2][13]};
#pragma unroll
            for (int d = 0; d < 4; ++d) { const float s = swap32(mm[d], hh); mm[d] = hh ? mm[d] : s; }
            const float M = fmaxf(fmaxf(mm[0], mm[1]), fmaxf(mm[2], mm[3]));
            acc *= __builtin_amdgcn_exp2f(mm[0] - M);
#pragma unroll
            for (int d = 0; d < 3; ++d) acc += p[d] * __builtin_amdgcn_exp2f(mm[d + 1] - M);
        }
        const float lsw = swap32(acc[12], hh);
        const float inv = __builtin_amdgcn_rcpf(hh ? acc[12] : lsw);
        acc *= inv;
        u32x4 lo = pack_lo(acc), hi = pack_hi(acc);
        if (hh) { hi[2] = 0u; hi[3] = 0u; }
#pragma unroll
        for (int i = 0; i < 4; ++i) { lo[i] = min(lo[i], SENT - 1u); hi[i] = min(hi[i], SENT - 1u); }
        __builtin_amdgcn_raw_buffer_store_b128(lo, rws, ooff, 0, SC1);
        __builtin_amdgcn_raw_buffer_store_b128(hi, rws, ooff + 512, 0, SC1);
        STAMP(11);
    } else {
        const u32x4 rx = mk_rsrc(x, SEQ * DM * 2), rw = mk_rsrc(w, DM * N3 * 2), rwt = mk_rsrc(w, h < 14 ? DM * N3 * 2 : 0);
        const unsigned vx = (lane >> 3) * 896 + (((lane & 7) ^ ((w4 & 1) * 4 + (lane >> 4))) * 16);
        const unsigned vw = (lane >> 3) * (N3 * 2) + 896 + (h < 14 ? h : 0) * 128 + (((lane & 7) ^ (((lane >> 4) & 1) << 2)) * 16);
        const unsigned vq = ((lane >> 2) * N3 + h * DH + (lane & 3) * 8) * 2;
        const unsigned xs0 = (128 * r + 8 * w4) * 896, ws0 = 8 * w4 * N3 * 2;
#define KG(p) ((p) + rot >= 7 ? (p) + rot - 7 : (p) + rot)
#define LDCHUNK(p) do { const unsigned kg = KG(p), sl = lds0 + ((p) % 6) * SLOT + w4 * 1024; \
            dma16(rx, vx, xs0 + kg * 128, sl); dma16(rwt, vw, ws0 + kg * 64 * N3 * 2, sl + 16384); dma16(rx, vx, xs0 + kg * 128 + 32 * 896, sl + 4096); \
            dma16(rx, vx, xs0 + kg * 128 + 64 * 896, sl + 8192); dma16(rwt, vw, ws0 + (kg * 64 + 32) * N3 * 2, sl + 16384 + 4096); dma16(rx, vx, xs0 + kg * 128 + 96 * 896, sl + 12288); } while (0)
        bf16x8 xf[28];
#define RDX(p) do { _Pragma("unroll") for (int s = 0; s < 4; ++s) xf[4 * (p) + s] = *(LAS const bf16x8*)(lds + ((p) % 6) * SLOT + (xa0 ^ (32 * s))); } while (0)
        LDCHUNK(0); LDCHUNK(1); LDCHUNK(2); LDCHUNK(3);
        STAMP(1);
        WAITV(18); BAR(); STAMP(2); LDCHUNK(4); RDX(0);
        WAITV(18); BAR(); LDCHUNK(5); RDX(1);
        WAITV(18); BAR(); LDCHUNK(6); RDX(2);
        WAITV(18); BAR(); STAMP(3);
#pragma unroll
        for (int p = 0; p < 7; ++p) dma16(rw, vq, (KG(p) * 64 + 16 * w4) * N3 * 2, lds0 + LDS_WQ + (4 * p + w4) * 1024);
        RDX(3);
        WAITV(19); BAR(); RDX(4);
        WAITV(13); BAR(); RDX(5);
        WAITV(7); BAR(); RDX(6);
        STAMP(4);
        WAITV(0); sync4(misc + 3, lane);
        STAMP(5);
        {
            LAS const char* qb = lds + LDS_WQ + (h8 + q4) * 64 + 32 * blk + 8 * p4;
            f32x16 aq = {};
            bf16x8 wq[28];
#pragma unroll
            for (int s = 0; s < 7; ++s) wq[s] = tr_frag(qb + 1024 * s, qb + 1024 * s + 256);
            SB();
#pragma unroll
            for (int s = 0; s < 28; ++s) { if (s + 7 < 28) wq[s + 7] = tr_frag(qb + 1024 * (s + 7), qb + 1024 * (s + 7) + 256); aq = MFMA32(wq[s], xf[s], aq); SB(); }
#pragma unroll
            for (int i = 0; i < 16; ++i) aq[i] *= QSCALE;
#pragma unroll
            for (int i = 12; i < 16; ++i) aq[i] = hh ? 0.f : aq[i];
            *(LAS u32x4*)(lds + LDS_Q + w4 * 2048 + lane * 16) = pack_lo(aq);
            *(LAS u32x4*)(lds + LDS_Q + w4 * 2048 + 1024 + lane * 16) = pack_hi(aq);
            float qn = 0.f;
#pragma unroll
            for (int i = 0; i < 16; ++i) qn = fmaf(aq[i], aq[i], qn);
            qn += swap32(qn, hh);
            lds_max(lds0 + LDS_MISC + 8, pos_bits(qn));
        }
        STAMP(6);
        BAR();
        STAMP(8);
        const u64 tag = (u64)misc[5] | ((u64)misc[6] << 32);
        const int cq = h & 3, rt = w4 & 1, kh = w4 >> 1, rb = 4 * r + (h >> 2);
        {
            const u32x4 rwo = mk_rsrc(wo, DM * DM * 2);
            unsigned vv[7];
#pragma unroll
            for (int k = 0; k < 7; ++k) { const int i = k * 64 + lane, row = i / 14, cc = i - row * 14; vv[k] = (row * DM + cq * 112 + cc * 8) * 2; }
            for (int grp = w4; grp < 14; grp += 4) {
#pragma unroll
                for (int k = 0; k < 7; ++k) { dma16(rwo, vv[k], grp * 32 * DM * 2, lds0 + LDS_WO + (grp * 7 + k) * 1024); __builtin_amdgcn_s_sleep(2); }
            }
        }
        WAITV(0); sync4(misc + 4, lane);
        const int g = lane >> 4, row0 = 16 * (g >> 1) + 4 * (g & 1) + q4;
        LAS const char* b1 = lds + LDS_WO + (kh * 8 * DH + row0) * 224 + 8 * p4;
        LAS const char* b2 = lds + LDS_WO + (kh * 8 * DH + row0 + (g == 3 ? 4 : 8)) * 224 + 8 * p4;
        bf16x8 wf[8][7];
#pragma unroll
        for (int i = 0; i < 7; ++i)
#pragma unroll
            for (int ct = 0; ct < 7; ++ct) { wf[i][ct] = tr_frag(b1 + i * 6272 + ct * 32, b2 + i * 6272 + ct * 32); PIN(wf[i][ct]); }
        GAS u64* fp = (GAS u64*)(ws + WS_FLAG + (r * 16 + (lane & 15)) * 256);
        for (unsigned spins = 0;; ++spins) {
            if (__all((__hip_atomic_load(fp, RLX_AGENT) >> 16) == tag) || spins > SPIN_CAP) break;
            __builtin_amdgcn_s_sleep(8);
        }
        STAMP(9);
        BAR();
        STAMP(10);
        u32x4 of[8];
        const unsigned oin = WS_O + ((2 * rb + rt) * 16 + 8 * kh) * 1024 + lane * 16;
        for (unsigned spins = 0;; ++spins) {
#pragma unroll
            for (int i = 0; i < 8; ++i) of[i] = __builtin_amdgcn_raw_buffer_load_b128(rws, oin + i * 1024, 0, SC1);
            unsigned mx = 0;
#pragma unroll
            for (int i = 0; i < 8; ++i) mx = max(max(mx, max(of[i][0], of[i][1])), max(of[i][2], of[i][3]));
            if (!__any(mx == SENT) || spins > SPIN_CAP) break;
            __builtin_amdgcn_s_sleep(2);
        }
        STAMP(12);
        f32x4 pa[7] = {};
#pragma unroll
        for (int ct = 0; ct < 7; ++ct) wf[7][ct] = tr_frag(b1 + 7 * 6272 + ct * 32, b2 + 7 * 6272 + ct * 32);
#pragma unroll
        for (int i = 0; i < 8; ++i)
#pragma unroll
            for (int ct = 0; ct < 7; ++ct) pa[ct] = MFMA16(wf[i][ct], BF(of[i]), pa[ct]);
        LAS char* ex = lds + LDS_Q + rt * 4096;
        LAS char* ey = lds + rt * 4096;
        BAR();
#pragma unroll
        for (int ct = 0; ct < 7; ++ct) { if (ct < 4 && kh == 1) *(LAS f32x4*)(ex + ct * 1024 + lane * 16) = pa[ct]; if (ct >= 4 && kh == 0) *(LAS f32x4*)(ey + (ct - 4) * 1024 + lane * 16) = pa[ct]; }
        BAR();
        __bf16* orow = out + (size_t)(32 * rb + 16 * rt + (lane & 15)) * DM + cq * 112 + 4 * g;
#pragma unroll
        for (int ct = 0; ct < 7; ++ct) {
            if (ct < 4 && kh == 0) *(bf16x4*)(orow + 16 * ct) = __builtin_convertvector(pa[ct] + *(LAS const f32x4*)(ex + ct * 1024 + lane * 16), bf16x4);
            if (ct >= 4 && kh == 1) *(bf16x4*)(orow + 16 * ct) = __builtin_convertvector(pa[ct] + *(LAS const f32x4*)(ey + (ct - 4) * 1024 + lane * 16), bf16x4);
        }
        if (bid == 0 && threadIdx.x == 256) __hip_atomic_store((GAS u64*)(ws + WS_EPOCH), tag, RLX_AGENT);
        STAMP(13);
    }
}

extern "C" void kernel_launch(void* const* d_in, const int* in_sizes, int n_in, void* d_out, int out_size, void* d_ws, size_t ws_size, hipStream_t stream) {
    (void)hipFuncSetAttribute((const void*)k_mha, hipFuncAttributeMaxDynamicSharedMemorySize, LDS_BYTES);
    k_mha<<<256, 512, LDS_BYTES, stream>>>((const __bf16*)d_in[0], (const __bf16*)d_in[1], (const __bf16*)d_in[2], (char*)d_ws, (__bf16*)d_out);
}
```

```cpp
#include <hip/hip_runtime.h>
#include <hip/hip_bf16.h>
#include <stdint.h>

typedef __bf16 bf16x8 __attribute__((ext_vector_type(8)));
typedef __bf16 bf16x4 __attribute__((ext_vector_type(4)));
typedef __bf16 bf16x2 __attribute__((ext_vector_type(2)));
typedef float f32x16 __attribute__((ext_vector_type(16)));
typedef float f32x8 __attribute__((ext_vector_type(8)));
typedef float f32x4 __attribute__((ext_vector_type(4)));
typedef float f32x2 __attribute__((ext_vector_type(2)));
typedef unsigned u32x4 __attribute__((ext_vector_type(4)));
typedef unsigned u32x2 __attribute__((ext_vector_type(2)));
typedef short s16x4 __attribute__((ext_vector_type(4)));
typedef unsigned long long u64;
#define LAS __attribute__((address_space(3)))
#define GAS __attribute__((address_space(1)))
#define DI __device__ __forceinline__
#define DI_ __device__ __forceinline__
#define MFMA32(a, b, c) __builtin_amdgcn_mfma_f32_32x32x16_bf16((a), (b), (c), 0, 0, 0)
#define MFMA16(a, b, c) __builtin_amdgcn_mfma_f32_16x16x32_bf16((a), (b), (c), 0, 0, 0)
#define SB() __builtin_amdgcn_sched_barrier(0)
#define PIN(x) asm volatile("" : "+v"(x))
#define MEMFENCE() asm volatile("" ::: "memory")
#define RLX_AGENT __ATOMIC_RELAXED, __HIP_MEMORY_SCOPE_AGENT
#define RLX_WG __ATOMIC_RELAXED, __HIP_MEMORY_SCOPE_WORKGROUP
#define SC1 16
#define BF(x) __builtin_bit_cast(bf16x8, x)
#define U4(x) __builtin_bit_cast(u32x4, x)
#define INCR 0
#define TAILV 0
#define XD 3

constexpr int SEQ = 2048, DM = 448, NH = 16, DH = 28, N3 = 1344;
constexpr float QSCALE = 0.18898223650461362f * 1.4426950408889634f;
constexpr unsigned TREC = 61440, TR_V = 28672;
constexpr unsigned WS_KV = 4096, WS_O = 4u << 20, WS_FLAG = 6u << 20, WS_OFLAG = WS_FLAG + 32768, WS_EPOCH = WS_OFLAG + 8192, WS_END = WS_EPOCH + 256;
constexpr int LDS_W = 0, LDS_X = 57344, XSLOT = 16384, LDS_WQ = LDS_X + XSLOT, LDS_WO = LDS_X, LDS_Q = 40960, LDS_MISC = 157696, LDS_XY = LDS_WO + 57344, LDS_BYTES = LDS_MISC + 64;
DI_ int cmb_slot(int k) { return 4096 * (k < 10 ? k : k + 2); }
constexpr float SAFE_T2 = 90.f * 90.f;
constexpr float SAFE_V2 = 1.0e12f;
constexpr unsigned SPIN_CAP = 1u << 18;
constexpr unsigned SENT = 0xFFFFFFFFu, OOB = 0xFFFFFF00u;

#define STAMP(i)
#define STAMPC(i)
#define STAMPV(i, v) (void)(v)
#define NST 0

DI bf16x8 tr_frag(LAS const char* p0, LAS const char* p1) {
    s16x4 a = __builtin_bit_cast(s16x4, __builtin_amdgcn_ds_read_tr16_b64_v4i16((LAS s16x4*)p0));
    s16x4 b = __builtin_bit_cast(s16x4, __builtin_amdgcn_ds_read_tr16_b64_v4i16((LAS s16x4*)p1));
    return __builtin_bit_cast(bf16x8, __builtin_shufflevector(a, b, 0, 1, 2, 3, 4, 5, 6, 7));
}
DI u32x4 pack_lo(const f32x16& c) { return U4(__builtin_convertvector(__builtin_shufflevector(c, c, 0, 1, 2, 3, 4, 5, 6, 7), bf16x8)); }
DI u32x4 pack_hi(const f32x16& c) { return U4(__builtin_convertvector(__builtin_shufflevector(c, c, 8, 9, 10, 11, 12, 13, 14, 15), bf16x8)); }
DI unsigned cvt2(float a, float b) { return __builtin_bit_cast(unsigned, __builtin_convertvector((f32x2){a, b}, bf16x2)); }
DI float swap32(float v, bool hi) {
    auto r = __builtin_amdgcn_permlane32_swap(__float_as_uint(v), __float_as_uint(v), false, false);
    return hi ? __uint_as_float(r[0]) : __uint_as_float(r[1]);
}
DI u32x4 piece(u32x4 v) {
    auto a = __builtin_amdgcn_permlane32_swap(v[0], v[2], false, false), b = __builtin_amdgcn_permlane32_swap(v[1], v[3], false, false);
    return (u32x4){a[0], b[0], a[1], b[1]};
}
DI float sumsq(u32x4 v) {
    float s = 0.f;
#pragma unroll
    for (int i = 0; i < 4; ++i) { const unsigned u = v[i]; const bf16x2 p = __builtin_bit_cast(bf16x2, u); s = __builtin_amdgcn_fdot2_f32_bf16(p, p, s, false); }
    return s;
}
DI u64 poll64(GAS u64* p) { u64 z = 0; asm volatile("" : "+v"(z)); return __hip_atomic_fetch_add(p, z, RLX_AGENT); }
DI unsigned pos_bits(float v) { return (v == v) ? __float_as_uint(v) : 0x7F800000u; }
DI void dma16(u32x4 rsrc, unsigned voff, unsigned soff, unsigned lds_dst) {
    unsigned keep;
    asm volatile("s_mov_b32 %0, m0\n\ts_mov_b32 m0, %4\n\ts_nop 0\n\tbuffer_load_dwordx4 %1, %2, %3 offen lds\n\ts_mov_b32 m0, %0"
                 : "=&s"(keep) : "v"(voff), "s"(rsrc), "s"(soff), "s"(lds_dst) : "memory");
}
DI u32x4 mk_rsrc(const void* p, unsigned bytes) { const u64 a = (u64)p; return (u32x4){(unsigned)a, (unsigned)(a >> 32) & 0xffffu, bytes, 0x00020000u}; }
#define WAITV(n) asm volatile("s_waitcnt vmcnt(%c0)" :: "i"(n) : "memory")
#define BAR() asm volatile("s_waitcnt lgkmcnt(0)\n\ts_barrier" ::: "memory")
DI void lds_max(unsigned addr, unsigned v) { asm volatile("ds_max_u32 %0, %1" :: "v"(addr), "v"(v) : "memory"); }
DI void wait4(LAS unsigned* cnt) { while (__builtin_amdgcn_readfirstlane(__hip_atomic_load(cnt, RLX_WG)) < 4u) __builtin_amdgcn_s_sleep(1); MEMFENCE(); }
DI void sync4(LAS unsigned* cnt, int lane) {
    asm volatile("s_waitcnt lgkmcnt(0)" ::: "memory");
    if (lane == 0) __hip_atomic_fetch_add(cnt, 1u, RLX_WG);
    while (__builtin_amdgcn_readfirstlane(__hip_atomic_load(cnt, RLX_WG)) < 4u) __builtin_amdgcn_s_sleep(1);
    MEMFENCE();
}

__global__ __launch_bounds__(512, 2) void k_mha(const __bf16* __restrict__ x, const __bf16* __restrict__ w, const __bf16* __restrict__ wo, char* ws, __bf16* __restrict__ out) {
    extern __shared__ __attribute__((aligned(16))) char lds_[];
    asm volatile("" :: "s"(x), "s"(w), "s"(wo), "s"(ws), "s"(out));
    LAS char* lds = (LAS char*)lds_;
    LAS unsigned* misc = (LAS unsigned*)(lds + LDS_MISC);
    const unsigned lds0 = (unsigned)(uintptr_t)lds;
    const int bid = blockIdx.x;
    const int h = (bid & 1) * 8 + ((bid >> 3) & 7), r = ((bid >> 1) & 3) * 4 + (bid >> 6);
    const int lane = threadIdx.x & 63, wid = __builtin_amdgcn_readfirstlane(threadIdx.x >> 6), w4 = wid & 3;
    const int c = lane & 31; const bool hh = lane >= 32;
    const int T = 4 * r + w4;
    const u64 t_entry = __builtin_amdgcn_s_memrealtime();
    const __amdgpu_buffer_rsrc_t rws = __builtin_amdgcn_make_buffer_rsrc(ws, 0, WS_END, 0x00020000);
    const int blk = (lane >> 4) & 1, q4 = (lane & 15) >> 2, p4 = lane & 3, h8 = hh ? 8 : 0;
    const unsigned xa0 = LDS_X + w4 * 4096 + c * 128 + (((hh ? 1 : 0) ^ ((c >> 1) & 7)) * 16);

    if (wid >= 4) {
        asm volatile("; role B" ::: "memory");
        __builtin_amdgcn_s_waitcnt(0x0F70);
        const u32x4 rw = mk_rsrc(w, DM * N3 * 2), rwt = mk_rsrc(w, h < 14 ? DM * N3 * 2 : 0);
        const unsigned vw = (lane >> 3) * (N3 * 2) + 896 + (h < 14 ? h : 0) * 128 + (((lane & 7) ^ (((lane >> 4) & 1) << 2)) * 16);
#define LDW(p) do { dma16(rwt, vw, (8 * (p) + w4) * 8 * N3 * 2, lds0 + LDS_W + (8 * (p) + w4) * 1024); dma16(rwt, vw, (8 * (p) + 4 + w4) * 8 * N3 * 2, lds0 + LDS_W + (8 * (p) + 4 + w4) * 1024); } while (0)
        LDW(0); LDW(1); LDW(2);
        STAMPV(0, t_entry); STAMP(1);
        const unsigned vq = ((lane >> 2) * N3 + ((7 * h) >> 1) * 8 + (lane & 3) * 8) * 2;
        bf16x8 xf[28];
#define RDX(p) do { _Pragma("unroll") for (int s = 0; s < 4; ++s) xf[4 * (p) + s] = *(LAS const bf16x8*)(lds + ((p) % 6) * XSLOT + (xa0 ^ (32 * s))); } while (0)
#define LDQ(p) dma16(rw, vq, ((p) * 64 + 16 * w4) * N3 * 2, lds0 + LDS_WQ + (4 * (p) + w4) * 1024)
        WAITV(4 + NST); BAR(); LDW(3); RDX(0);
        WAITV(4 + NST); BAR(); LDW(4); RDX(1);
        WAITV(4 + NST); BAR(); LDW(5); RDX(2);
        WAITV(4); BAR(); LDW(6); LDQ(0); LDQ(1); LDQ(2); RDX(3);
        WAITV(7); BAR(); LDQ(3); LDQ(4); RDX(4);
        WAITV(7); BAR(); LDQ(5); LDQ(6); RDX(5);
        WAITV(7); BAR(); RDX(6);
        STAMP(4);
        WAITV(0); sync4(misc + 3, lane);
        STAMP(5);
        {
            LAS const char* qb = lds + LDS_WQ + (h8 + q4) * 64 + 32 * blk + 8 * p4;
            f32x16 aq = {};
            bf16x8 wq[28];
#pragma unroll
            for (int s = 0; s < 6; ++s) wq[s] = tr_frag(qb + 1024 * s, qb + 1024 * s + 256);
            SB();
#pragma unroll
            for (int s = 0; s < 28; ++s) { aq = MFMA32(wq[s], xf[s], aq); SB(); if (s + 6 < 28) wq[s + 6] = tr_frag(qb + 1024 * (s + 6), qb + 1024 * (s + 6) + 256); SB(); }
#pragma unroll
            for (int i = 0; i < 16; ++i) aq[i] *= QSCALE;
#pragma unroll
            for (int i = 0; i < 4; ++i) { aq[12 + i] = (hh && !(h & 1)) ? 0.f : aq[12 + i]; aq[i] = (!hh && (h & 1)) ? 0.f : aq[i]; }
            *(LAS u32x4*)(lds + LDS_Q + w4 * 2048 + lane * 16) = piece(pack_lo(aq));
            *(LAS u32x4*)(lds + LDS_Q + w4 * 2048 + 1024 + lane * 16) = piece(pack_hi(aq));
            float qn = 0.f;
#pragma unroll
            for (int i = 0; i < 16; ++i) qn = fmaf(aq[i], aq[i], qn);
            qn += swap32(qn, hh);
            lds_max(lds0 + LDS_MISC + 8, pos_bits(qn));
        }
        STAMP(6);
        sync4(misc + 7, lane);
        if (h >= 14) __builtin_amdgcn_endpgm();
        const int c3 = h >= 7 ? h - 7 : h, kh = w4 >> 1, T3 = 4 * r + (h >= 7 ? 2 : 0) + (w4 & 1);
        wait4(misc);
        STAMP(8);
        {
            const u32x4 rwo = mk_rsrc(wo, DM * DM * 2);
            const unsigned vo = (lane >> 3) * (DM * 2) + c3 * 128 + (((lane & 7) ^ (((lane >> 4) & 1) << 2)) * 16);
#pragma unroll
            for (int i = 0; i < 14; ++i) dma16(rwo, vo, (w4 + 4 * i) * 8 * DM * 2, lds0 + LDS_WO + (w4 + 4 * i) * 1024);
        }
        const u64 tag = (u64)misc[5] | ((u64)misc[6] << 32);
        const int sw = (q4 >> 1) & 1;
        unsigned b1 = LDS_WO + (kh * 8 * DH + (hh ? 4 : 0) + q4) * 128 + 64 * (sw ^ kh) + 32 * blk + 8 * p4;
        unsigned b3 = b1 + (hh ? 20 : 24) * 128;
        unsigned oin = WS_O + (T3 * 16 + 8 * kh) * 2048 + lane * 16;
        GAS u64* fp = (GAS u64*)(ws + WS_OFLAG + (T3 * 16 + 8 * kh + (lane & 7)) * 8);
        LAS char* exs = lds + LDS_XY + ((w4 & 1) * 2 + (kh ^ 1)) * 4096 + lane * 16;
        LAS const char* exr = lds + LDS_XY + ((w4 & 1) * 2 + kh) * 4096 + lane * 16;
        GAS __bf16* orow = (GAS __bf16*)out + (size_t)(32 * T3 + c) * DM + c3 * 64 + kh * 32 + h8;
        PIN(orow); PIN(fp); PIN(oin); PIN(b1); PIN(b3); PIN(exs); PIN(exr);
        if (bid == 0 && w4 == 0) {
            GAS u64* sp = (GAS u64*)(ws + WS_FLAG + (((lane >> 3) & 3) * 256 + 14) * 32 + (lane & 7) * 8);
            for (unsigned spins = 0;; ++spins) { if (__all((__hip_atomic_load(sp, RLX_AGENT) >> 16) == tag) || spins > SPIN_CAP) break; __builtin_amdgcn_s_sleep(8); }
        }
        f32x16 pa[2] = {};
        bf16x8 wf[8][2][2];
        STAMP(9);
        WAITV(0); BAR();
        STAMP(10);
        {
#pragma unroll
            for (int i = 0; i < 8; ++i)
#pragma unroll
                for (int a = 0; a < 2; ++a) {
                    LAS const char *p1 = lds + (b1 ^ (64 * a)), *p3 = lds + (b3 ^ (64 * a));
                    wf[i][0][a] = tr_frag(p1 + i * 3584, p1 + i * 3584 + 1024); wf[i][1][a] = tr_frag(p1 + i * 3584 + 2048, p3 + i * 3584);
                    PIN(wf[i][0][a]); PIN(wf[i][1][a]); }
            u64 fv = tag;
            for (unsigned spins = 0;; ++spins) {
                if (lane < 8) fv = poll64(fp);
                if (__all(fv == tag) || spins > SPIN_CAP) break;
            }
            STAMP(11);
            for (unsigned spins = 0;; ++spins) {
                MEMFENCE();
                u32x4 of[8][2];
#pragma unroll
                for (int i = 0; i < 8; ++i) { of[i][0] = __builtin_amdgcn_raw_buffer_load_b128(rws, oin, i * 2048, SC1); SB(); of[i][1] = __builtin_amdgcn_raw_buffer_load_b128(rws, oin + 1024, i * 2048, SC1); SB(); }
                unsigned mx = 0;
                pa[0] = (f32x16){}; pa[1] = (f32x16){};
#pragma unroll
                for (int i = 0; i < 8; ++i)
#pragma unroll
                    for (int k = 0; k < 2; ++k) {
                        pa[1] = MFMA32(wf[i][k][1], BF(of[i][k]), pa[1]); SB();
                        mx = max(max(mx, of[i][k][0]), of[i][k][1]); mx = max(max(mx, of[i][k][2]), of[i][k][3]); PIN(mx); SB();
                        pa[0] = MFMA32(wf[i][k][0], BF(of[i][k]), pa[0]); SB();
                    }
                if (!__any(mx == SENT) || spins > SPIN_CAP) break;
            }
        }
        STAMP(12);
        {
#pragma unroll
            for (int g = 0; g < 4; ++g) *(LAS f32x4*)(exs + g * 1024) = (f32x4){pa[1][4 * g], pa[1][4 * g + 1], pa[1][4 * g + 2], pa[1][4 * g + 3]};
            BAR();
#pragma unroll
            for (int g = 0; g < 4; ++g) { const f32x4 v = *(LAS const f32x4*)(exr + g * 1024);
#pragma unroll
                for (int i = 0; i < 4; ++i) pa[0][4 * g + i] += v[i]; }
            *(GAS u32x4*)(orow) = piece(pack_lo(pa[0]));
            *(GAS u32x4*)(orow + 16) = piece(pack_hi(pa[0]));
        }
        if (bid == 0 && threadIdx.x == 256) __hip_atomic_store((GAS u64*)(ws + WS_EPOCH), tag, RLX_AGENT);
        STAMP(13);
        __builtin_amdgcn_endpgm();
    } else {
        const u32x4 rx = mk_rsrc(x, SEQ * DM * 2);
        const unsigned vx = (lane >> 3) * 896 + (((lane & 7) ^ (lane >> 4)) * 16), vx1 = vx ^ 64;
        const unsigned xs0 = 32 * T * 896;
#define LDX(p) do { const unsigned sl = lds0 + LDS_X + ((p) % 6) * XSLOT + w4 * 4096; \
            dma16(rx, vx, xs0 + (p) * 128, sl); dma16(rx, vx1, xs0 + (p) * 128 + 8 * 896, sl + 1024); dma16(rx, vx, xs0 + (p) * 128 + 16 * 896, sl + 2048); dma16(rx, vx1, xs0 + (p) * 128 + 24 * 896, sl + 3072); } while (0)
        LDX(0); LDX(1); LDX(2);
        SB();
        u64 epoch;
        asm volatile("global_load_dwordx2 %0, %1, %2 sc1" : "=v"(epoch) : "v"(WS_EPOCH), "s"(ws) : "memory");
#define NPT 0
        if (threadIdx.x < 12) misc[threadIdx.x] = 0u;
        const unsigned ooff = WS_O + (T * 16 + h) * 2048 + lane * 16;
        __builtin_amdgcn_raw_buffer_store_b128((u32x4){SENT, SENT, SENT, SENT}, rws, ooff, 0, SC1);
        __builtin_amdgcn_raw_buffer_store_b128((u32x4){SENT, SENT, SENT, SENT}, rws, ooff + 1024, 0, SC1);
        SB();
        const bool isk = h < 7, isv = h >= 7 && h < 14;
        const int ct = isk ? h : h - 7;
        unsigned dst0, dst1, dstp;
        { const int c0 = 64 * ct + c, h0 = (c0 * 293) >> 13, c1 = c0 + 32, h1 = (c1 * 293) >> 13;
          dst0 = WS_KV + T * TREC + (isk ? ct * 4096 + lane * 16 : TR_V + h0 * 2048 + (c0 - DH * h0 + (hh ? 32 : 0)) * 16);
          dst1 = WS_KV + T * TREC + TR_V + h1 * 2048 + (c1 - DH * h1 + (hh ? 32 : 0)) * 16;
          const int hp = (64 * ct) / DH + (lane >> 4);
          dstp = (DH * hp + DH - 1 < 64 * ct + 64) ? WS_KV + T * TREC + TR_V + hp * 2048 + ((lane >> 2) & 3) * 512 + (DH + (lane & 3)) * 16 : OOB;
          PIN(dst0); PIN(dst1); PIN(dstp); }
        const int sw = (q4 >> 1) & 1;
        const unsigned wa0 = LDS_W + (h8 + q4) * 128 + 64 * sw + 32 * blk + 8 * p4, wa1 = wa0 ^ 64;
        f32x16 a0 = {}, a1 = {};
        bf16x8 xf_[2][4], w0_[2][4], w1_[2][4];
        STAMPV(0, t_entry); STAMP(1);
#define XW(p) ((p) <= 2 ? 11 + NPT + NST : (p) <= 4 ? 8 : 4 * (6 - (p)))
#define P1RD1(p, s) do { xf_[(p) & 1][s] = *(LAS const bf16x8*)(lds + ((p) % 6) * XSLOT + (xa0 ^ (32 * (s)))); \
            w0_[(p) & 1][s] = tr_frag(lds + wa0 + 8192 * (p) + 2048 * (s), lds + wa0 + 8192 * (p) + 2048 * (s) + 512); } while (0)
#define P1RD2(p, s) do { w1_[(p) & 1][s] = tr_frag(lds + wa1 + 8192 * (p) + 2048 * (s), lds + wa1 + 8192 * (p) + 2048 * (s) + 512); } while (0)
#define P1M(acc, wv, p, s, KT) do { if (KT) acc = MFMA32(wv[(p) & 1][s], xf_[(p) & 1][s], acc); else acc = MFMA32(xf_[(p) & 1][s], wv[(p) & 1][s], acc); } while (0)
#define P1X(p, s) (xf_[(p) & 1][s] = *(LAS const bf16x8*)(lds + ((p) % 6) * XSLOT + (xa0 ^ (32 * (s)))))
#define P1W(wv, wa, p, s) (wv[(p) & 1][s] = tr_frag(lds + wa + 8192 * (p) + 2048 * (s), lds + wa + 8192 * (p) + 2048 * (s) + 512))
#define P1MM(acc, wv, p, s, KT) do { if ((p) > 0) P1M(acc, wv, (p) - 1, s, KT); SB(); } while (0)
#define P1STEP(p, KT) do { WAITV(XW(p)); BAR(); SB(); if (KT < 2) { \
            P1MM(a0, w0_, p, 0, KT); P1X(p, 0); P1W(w0_, wa0, p, 0); SB(); P1MM(a1, w1_, p, 0, KT); P1W(w1_, wa1, p, 0); P1X(p, 1); SB(); \
            P1MM(a0, w0_, p, 1, KT); P1W(w0_, wa0, p, 1); P1W(w1_, wa1, p, 1); SB(); P1MM(a1, w1_, p, 1, KT); P1X(p, 2); P1W(w0_, wa0, p, 2); SB(); \
            P1MM(a0, w0_, p, 2, KT); P1W(w1_, wa1, p, 2); P1X(p, 3); SB(); P1MM(a1, w1_, p, 2, KT); P1W(w0_, wa0, p, 3); P1W(w1_, wa1, p, 3); SB(); \
            P1MM(a0, w0_, p, 3, KT); } if ((p) <= 3) LDX((p) + 3); SB(); if (KT < 2) P1MM(a1, w1_, p, 3, KT); } while (0)
#define P1LAST(KT) do { _Pragma("unroll") for (int s = 0; s < 4; ++s) { P1M(a0, w0_, 6, s, KT); P1M(a1, w1_, 6, s, KT); } SB(); } while (0)
#define P1ALL(KT) do { P1STEP(0, KT); P1STEP(1, KT); P1STEP(2, KT); P1STEP(3, KT); P1STEP(4, KT); P1STEP(5, KT); P1STEP(6, KT); } while (0)
        float stat = 0.f;
        if (isk) {
            P1ALL(1); STAMP(3); P1LAST(1);
            STAMP(4);
            const u32x4 k0 = piece(pack_lo(a0)), k1 = piece(pack_hi(a0)), k2 = piece(pack_lo(a1)), k3 = piece(pack_hi(a1));
            __builtin_amdgcn_raw_buffer_store_b128(k0, rws, dst0, 0, SC1); __builtin_amdgcn_raw_buffer_store_b128(k1, rws, dst0 + 1024, 0, SC1);
            __builtin_amdgcn_raw_buffer_store_b128(k2, rws, dst0 + 2048, 0, SC1); __builtin_amdgcn_raw_buffer_store_b128(k3, rws, dst0 + 3072, 0, SC1);
            SB();
            stat = (sumsq(k0) + sumsq(k1)) + (sumsq(k2) + sumsq(k3));
            stat += swap32(stat, hh);
        } else if (isv) {
            P1ALL(0); STAMP(3); P1LAST(0);
            STAMP(4);
            const u32x4 k0 = pack_lo(a0), k1 = pack_hi(a0), k2 = pack_lo(a1), k3 = pack_hi(a1);
            __builtin_amdgcn_raw_buffer_store_b128(k0, rws, dst0, 0, SC1); __builtin_amdgcn_raw_buffer_store_b128(k1, rws, dst0 + 1024, 0, SC1);
            __builtin_amdgcn_raw_buffer_store_b128(k2, rws, dst1, 0, SC1); __builtin_amdgcn_raw_buffer_store_b128(k3, rws, dst1 + 1024, 0, SC1);
            const unsigned one = (lane & 3) == 0 ? 0x3F803F80u : 0u;
            __builtin_amdgcn_raw_buffer_store_b128((u32x4){one, one, one, one}, rws, dstp, 0, SC1);
            SB();
            stat = (sumsq(k0) + sumsq(k1)) + (sumsq(k2) + sumsq(k3));
        } else {
            P1ALL(2); STAMP(3); STAMP(4);
        }
        lds_max(lds0 + LDS_MISC + 4, pos_bits(stat));
        asm volatile("" : "+v"(epoch));
        const u64 tag = (((u64)__builtin_amdgcn_readfirstlane((unsigned)(epoch >> 32)) << 32 | (unsigned)__builtin_amdgcn_readfirstlane((unsigned)epoch)) + 1ull) & 0xFFFFFFFFFFFFull;
        if (threadIdx.x == 0) { misc[5] = (unsigned)tag; misc[6] = (unsigned)(tag >> 32); }
        GAS u64* fo = (GAS u64*)(ws + WS_FLAG + (((r >> 2) * 16 + (lane & 15)) * 16 + h) * 32 + (r & 3) * 8);
        PIN(fo);
        STAMP(5);
        asm volatile("s_waitcnt vmcnt(0) lgkmcnt(0)" ::: "memory");
        {
            unsigned arr = 0;
            if (lane == 0) arr = __hip_atomic_fetch_add(misc, 1u, RLX_WG);
            if (__builtin_amdgcn_readfirstlane(arr) == 3u && lane < 16) {
                const unsigned sb = (__hip_atomic_load(misc + 1, RLX_WG) + 0xFFFFu) >> 16;
                (void)__hip_atomic_exchange(fo, tag << 16 | sb, RLX_AGENT);
            }
        }
        STAMP(6);
        const int pc0 = (7 * h) >> 1, ka = pc0 >> 3, kb = (pc0 + 3) >> 3, va = (DH * h) >> 6, vb = (DH * h + DH - 1) >> 6;
        const int l4 = (lane >> 2) & 3;
        GAS u64* fp = (GAS u64*)(ws + WS_FLAG + ((w4 * 16 + r) * 16 + (l4 == 0 ? ka : l4 == 1 ? kb : l4 == 2 ? 7 + va : 7 + vb)) * 32 + (lane & 3) * 8);
        u64 fv = tag << 16;
        if (lane < 16) fv = poll64(fp);
        SB();
        f32x16 o[4] = {};
        PIN(o[0]); PIN(o[1]); PIN(o[2]); PIN(o[3]);
        wait4(misc + 7);
        bf16x8 qf[4][2];
#pragma unroll
        for (int t = 0; t < 4; ++t) { const int tg = (w4 + 1 + t) & 3; qf[t][0] = *(LAS const bf16x8*)(lds + LDS_Q + tg * 2048 + lane * 16); qf[t][1] = *(LAS const bf16x8*)(lds + LDS_Q + tg * 2048 + 1024 + lane * 16); }
        const float qn = __uint_as_float(misc[2]);
        STAMP(8);
        LAS char* cmb[3];
#pragma unroll
        for (int t = 0; t < 3; ++t) cmb[t] = lds + cmb_slot(((w4 + 1 + t) & 3) * 3 + 2 - t) + lane * 16;
#define SENDQ(t, g) (*(LAS f32x4*)(cmb[t] + (g) * 1024) = (f32x4){o[t][4 * (g)], o[t][4 * (g) + 1], o[t][4 * (g) + 2], o[t][4 * (g) + 3]})
#define SEND(t, mv) do { o[t][13] = hh ? (mv) : o[t][13]; SENDQ(t, 0); SENDQ(t, 1); SENDQ(t, 2); SENDQ(t, 3); } while (0)
        for (unsigned spins = 0; !__all((fv >> 16) == tag) && spins <= SPIN_CAP; ++spins) { if (lane < 16) fv = poll64(fp); }
        MEMFENCE();
        STAMP(7);
        const __amdgpu_buffer_rsrc_t rk = __builtin_amdgcn_make_buffer_rsrc(ws + WS_KV + 16 * w4 * TREC + pc0 * 512, 16, 16 * TREC, 1 << 23);
        const __amdgpu_buffer_rsrc_t rv = __builtin_amdgcn_make_buffer_rsrc(ws + WS_KV + 16 * w4 * TREC + TR_V + h * 2048, 16, 16 * TREC, 1 << 23);
#define LDF(rs_, f_, so_) __builtin_amdgcn_raw_buffer_load_b128(rs_, (f_) * 1024, so_, SC1)
        u32x4 kf[4][2], vf[4][2];
#pragma unroll
        for (int b = 0; b < 4; ++b) { kf[b][0] = LDF(rk, 0, b * TREC); SB(); kf[b][1] = LDF(rk, 1, b * TREC); SB();
            if (b < 3) { vf[b][0] = LDF(rv, 0, b * TREC); SB(); vf[b][1] = LDF(rv, 1, b * TREC); SB(); } }
        vf[3][0] = (u32x4){0u, 0u, 0u, 0u}; vf[3][1] = vf[3][0];
        SB();
        unsigned sa_ = 0, sb_ = 0, sv_ = 0;
        { const unsigned s16 = (unsigned)fv << 16;
#pragma unroll
          for (int i = 0; i < 4; ++i) { sa_ = max(sa_, (unsigned)__builtin_amdgcn_readlane(s16, i)); sb_ = max(sb_, (unsigned)__builtin_amdgcn_readlane(s16, 4 + i));
              sv_ = max(sv_, max((unsigned)__builtin_amdgcn_readlane(s16, 8 + i), (unsigned)__builtin_amdgcn_readlane(s16, 12 + i))); } }
        const float kn = __uint_as_float(sa_) + (ka != kb ? __uint_as_float(sb_) : 0.f);
        int fasti = (kn * qn * 1.05f <= SAFE_T2 && __uint_as_float(sv_) <= SAFE_V2) ? 1 : 0;
        const bool fast = __builtin_amdgcn_readfirstlane(fasti) != 0;
        float m[4] = {0.f, 0.f, 0.f, 0.f};
        STAMP(9); STAMPC(14);
        if (__builtin_expect(fast, 1)) {
            f32x16 sa, sb = {};
            u32x4 plo = {}, phi = {};
            const f32x16 zero = {};
            sa = MFMA32(BF(kf[0][0]), qf[0][0], zero); sa = MFMA32(BF(kf[0][1]), qf[0][1], sa);
            SB();
#define EX(S, i) S[i] = __builtin_amdgcn_exp2f(S[i])
#define STEP(SC, SN, B, t, X1, X2, X3, X4) do { \
            constexpr int tn = ((t) + 1) & 3, bn = (t) == 3 ? ((B) + 1) & 3 : (B), tp = ((t) + 3) & 3, bp = (t) == 0 ? ((B) + 3) & 3 : (B); \
            o[tp] = MFMA32(BF(vf[bp][0]), BF(plo), o[tp]); SB(); X1; SB(); \
            phi[2] = cvt2(SN[12], SN[13]); phi[3] = cvt2(SN[14], SN[15]); PIN(phi); SB(); EX(SC, 0); EX(SC, 1); EX(SC, 2); EX(SC, 3); PIN(SC); SB(); \
            SN = MFMA32(BF(kf[bn][0]), qf[tn][0], zero); SB(); X2; SB(); \
            plo[0] = cvt2(SC[0], SC[1]); plo[1] = cvt2(SC[2], SC[3]); PIN(plo); SB(); EX(SC, 4); EX(SC, 5); EX(SC, 6); EX(SC, 7); PIN(SC); SB(); \
            SN = MFMA32(BF(kf[bn][1]), qf[tn][1], SN); SB(); X3; SB(); \
            plo[2] = cvt2(SC[4], SC[5]); plo[3] = cvt2(SC[6], SC[7]); PIN(plo); SB(); EX(SC, 8); EX(SC, 9); EX(SC, 10); EX(SC, 11); PIN(SC); SB(); \
            o[tp] = MFMA32(BF(vf[bp][1]), BF(phi), o[tp]); SB(); X4; SB(); \
            phi[0] = cvt2(SC[8], SC[9]); phi[1] = cvt2(SC[10], SC[11]); PIN(phi); SB(); EX(SC, 12); EX(SC, 13); EX(SC, 14); EX(SC, 15); PIN(SC); SB(); \
            } while (0)
#define NOP (void)0
#define PIN4(a, b, c_, d) asm volatile("" : "+v"(a), "+v"(b), "+v"(c_), "+v"(d))
#define PINS(x) asm volatile("" : "+s"(x))
#define BLOCK(B) \
            STEP(sa, sb, B, 0, vf[((B) + 3) & 3][0] = LDF(rv, 0, so), PIN4(vf[B][0], vf[B][1], kf[((B) + 1) & 3][0], kf[((B) + 1) & 3][1]), NOP, vf[((B) + 3) & 3][1] = LDF(rv, 1, so)); \
            STEP(sb, sa, B, 1, do { so += TREC; PINS(so); } while (0), NOP, NOP, NOP); \
            STEP(sa, sb, B, 2, NOP, kf[B][0] = LDF(rk, 0, so), kf[B][1] = LDF(rk, 1, so), NOP); \
            STEP(sb, sa, B, 3, NOP, NOP, NOP, NOP)
            unsigned so = 3 * TREC;
            for (int it = 0; it < 3; ++it) { BLOCK(0); BLOCK(1); BLOCK(2); BLOCK(3); }
            STEP(sa, sb, 0, 0, vf[3][0] = LDF(rv, 0, so), PIN4(vf[0][0], vf[0][1], kf[1][0], kf[1][1]), NOP, vf[3][1] = LDF(rv, 1, so));
            STEP(sb, sa, 0, 1, NOP, NOP, NOP, NOP); STEP(sa, sb, 0, 2, NOP, NOP, NOP, NOP); STEP(sb, sa, 0, 3, NOP, NOP, NOP, NOP);
            STEP(sa, sb, 1, 0, NOP, PIN4(vf[1][0], vf[1][1], kf[2][0], kf[2][1]), NOP, NOP); STEP(sb, sa, 1, 1, NOP, NOP, NOP, NOP); STEP(sa, sb, 1, 2, NOP, NOP, NOP, NOP); STEP(sb, sa, 1, 3, NOP, NOP, NOP, NOP);
            STEP(sa, sb, 2, 0, NOP, PIN4(vf[2][0], vf[2][1], kf[3][0], kf[3][1]), NOP, NOP); STEP(sb, sa, 2, 1, NOP, NOP, NOP, NOP); STEP(sa, sb, 2, 2, NOP, NOP, NOP, NOP); STEP(sb, sa, 2, 3, NOP, NOP, NOP, NOP);
            STEP(sa, sb, 3, 0, NOP, PIN4(vf[3][0], vf[3][1], kf[3][0], kf[3][1]), NOP, NOP); STEP(sb, sa, 3, 1, NOP, NOP, NOP, NOP);
            STEP(sa, sb, 3, 2, NOP, SENDQ(0, 0), SENDQ(0, 1), SENDQ(0, 2));
            STEP(sb, sa, 3, 3, SENDQ(0, 3), SENDQ(1, 0), SENDQ(1, 1), SENDQ(1, 2));
            o[3] = MFMA32(BF(vf[3][0]), BF(plo), o[3]); SB(); SENDQ(1, 3); SB();
            phi[2] = cvt2(sb[12], sb[13]); phi[3] = cvt2(sb[14], sb[15]); SB();
            SENDQ(2, 0); SB();
            o[3] = MFMA32(BF(vf[3][1]), BF(phi), o[3]); SB();
            SENDQ(2, 1); SENDQ(2, 2); SENDQ(2, 3);
        }
        PIN(fasti);
        if (__builtin_expect(__builtin_amdgcn_readfirstlane(fasti) == 0, 0)) {
#pragma unroll
            for (int t = 0; t < 4; ++t) m[t] = -INFINITY;
            for (int j = 0; j < 16; ++j) {
                kf[0][0] = LDF(rk, 0, j * TREC); kf[0][1] = LDF(rk, 1, j * TREC); vf[0][0] = LDF(rv, 0, j * TREC); vf[0][1] = LDF(rv, 1, j * TREC);
#pragma unroll
                for (int t = 0; t < 4; ++t) {
                    SB();
                    f32x16 s = {};
                    s = MFMA32(BF(kf[0][0]), qf[t][0], s);
                    s = MFMA32(BF(kf[0][1]), qf[t][1], s);
                    float bm = s[0];
#pragma unroll
                    for (int i = 1; i < 16; ++i) bm = fmaxf(bm, s[i]);
                    bm = fmaxf(bm, swap32(bm, hh));
                    const float mn = fmaxf(m[t], bm), al = __builtin_amdgcn_exp2f(m[t] - mn);
                    m[t] = mn;
                    o[t] *= al; PIN(o[t]); SB();
#pragma unroll
                    for (int i = 0; i < 16; ++i) s[i] = __builtin_amdgcn_exp2f(s[i] - mn);
                    PIN(s); SB();
                    o[t] = MFMA32(BF(vf[0][0]), BF(pack_lo(s)), o[t]);
                    o[t] = MFMA32(BF(vf[0][1]), BF(pack_hi(s)), o[t]);
                    PIN(o[t]);
                }
            }
            SEND(0, m[0]); SEND(1, m[1]); SEND(2, m[2]);
            o[3][13] = hh ? m[3] : o[3][13];
        }
        STAMPC(15); STAMP(10);
        f32x16 acc = o[3];
        BAR();
        f32x16 p[3];
#pragma unroll
        for (int d = 0; d < 3; ++d)
#pragma unroll
            for (int g = 0; g < 4; ++g) { const f32x4 v = *(LAS const f32x4*)(lds + cmb_slot(w4 * 3 + d) + g * 1024 + lane * 16);
#pragma unroll
                for (int i = 0; i < 4; ++i) p[d][4 * g + i] = v[i]; }
        const bool plain = __all(!hh || (acc[13] == 0.f && p[0][13] == 0.f && p[1][13] == 0.f && p[2][13] == 0.f));
        if (__builtin_expect(plain, 1)) {
            acc = (acc + p[0]) + (p[1] + p[2]);
        } else {
            float mm[4] = {acc[13], p[0][13], p[1][13], p[2][13]};
#pragma unroll
            for (int d = 0; d < 4; ++d) { const float s = swap32(mm[d], hh); mm[d] = hh ? mm[d] : s; }
            const float M = fmaxf(fmaxf(mm[0], mm[1]), fmaxf(mm[2], mm[3]));
            acc *= __builtin_amdgcn_exp2f(mm[0] - M);
#pragma unroll
            for (int d = 0; d < 3; ++d) acc += p[d] * __builtin_amdgcn_exp2f(mm[d + 1] - M);
        }
        const float lsw = swap32(acc[12], hh);
        const float inv = __builtin_amdgcn_rcpf(hh ? acc[12] : lsw);
        acc *= inv;
        u32x4 lo = pack_lo(acc), hi = pack_hi(acc);
        if (hh) { hi[2] = 0u; hi[3] = 0u; }
#pragma unroll
        for (int i = 0; i < 4; ++i) { lo[i] = min(lo[i], SENT - 1u); hi[i] = min(hi[i], SENT - 1u); }
        __builtin_amdgcn_raw_buffer_store_b128(lo, rws, ooff, 0, SC1);
        __builtin_amdgcn_raw_buffer_store_b128(hi, rws, ooff + 1024, 0, SC1);
        MEMFENCE();
        if (lane == 0) (void)__hip_atomic_exchange((GAS u64*)(ws + WS_OFLAG + (T * 16 + h) * 8), tag, RLX_AGENT);
        STAMP(11);
        __builtin_amdgcn_endpgm();
    }
}

extern "C" void kernel_launch(void* const* d_in, const int* in_sizes, int n_in, void* d_out, int out_size, void* d_ws, size_t ws_size, hipStream_t stream) {
    (void)hipFuncSetAttribute((const void*)k_mha, hipFuncAttributeMaxDynamicSharedMemorySize, LDS_BYTES);
    k_mha<<<256, 512, LDS_BYTES, stream>>>((const __bf16*)d_in[0], (const __bf16*)d_in[1], (const __bf16*)d_in[2], (char*)d_ws, (__bf16*)d_out);
}
```

```cpp
#include <hip/hip_runtime.h>
#include <hip/hip_bf16.h>
#include <stdint.h>

typedef __bf16 bf16x8 __attribute__((ext_vector_type(8)));
typedef __bf16 bf16x4 __attribute__((ext_vector_type(4)));
typedef __bf16 bf16x2 __attribute__((ext_vector_type(2)));
typedef float f32x16 __attribute__((ext_vector_type(16)));
typedef float f32x8 __attribute__((ext_vector_type(8)));
typedef float f32x4 __attribute__((ext_vector_type(4)));
typedef float f32x2 __attribute__((ext_vector_type(2)));
typedef unsigned u32x4 __attribute__((ext_vector_type(4)));
typedef unsigned u32x2 __attribute__((ext_vector_type(2)));
typedef short s16x4 __attribute__((ext_vector_type(4)));
typedef unsigned long long u64;
#define LAS __attribute__((address_space(3)))
#define GAS __attribute__((address_space(1)))
#define DI __device__ __forceinline__
#define DI_ __device__ __forceinline__
#define MFMA32(a, b, c) __builtin_amdgcn_mfma_f32_32x32x16_bf16((a), (b), (c), 0, 0, 0)
#define MFMA16(a, b, c) __builtin_amdgcn_mfma_f32_16x16x32_bf16((a), (b), (c), 0, 0, 0)
#define SB() __builtin_amdgcn_sched_barrier(0)
#define PIN(x) asm volatile("" : "+v"(x))
#define MEMFENCE() asm volatile("" ::: "memory")
#define RLX_AGENT __ATOMIC_RELAXED, __HIP_MEMORY_SCOPE_AGENT
#define RLX_WG __ATOMIC_RELAXED, __HIP_MEMORY_SCOPE_WORKGROUP
#define SC1 16
#define BF(x) __builtin_bit_cast(bf16x8, x)
#define U4(x) __builtin_bit_cast(u32x4, x)
#define INCR 0
#define TAILV 0
#define XD 3

constexpr int SEQ = 2048, DM = 448, NH = 16, DH = 28, N3 = 1344;
constexpr float QSCALE = 0.18898223650461362f * 1.4426950408889634f;
constexpr unsigned TREC = 61440, TR_V = 28672;
constexpr unsigned WS_KV = 4096, WS_O = 4u << 20, WS_FLAG = 6u << 20, WS_OFLAG = WS_FLAG + 32768, WS_EPOCH = WS_OFLAG + 8192, WS_END = WS_EPOCH + 256;
constexpr int LDS_W = 0, LDS_X = 57344, XSLOT = 16384, LDS_WQ = LDS_X + XSLOT, LDS_WO = LDS_X, LDS_Q = 40960, LDS_MISC = 157696, LDS_XY = LDS_WO + 57344, LDS_BYTES = LDS_MISC + 64;
DI_ int cmb_slot(int k) { return 4096 * (k < 10 ? k : k + 2); }
constexpr float SAFE_T2 = 90.f * 90.f;
constexpr float SAFE_V2 = 1.0e12f;
constexpr unsigned SPIN_CAP = 1u << 18;
constexpr unsigned SENT = 0xFFFFFFFFu, OOB = 0xFFFFFF00u;

#define STAMP(i)
#define STAMPC(i)
#define STAMPV(i, v) (void)(v)
#define NST 0

DI bf16x8 tr_frag(LAS const char* p0, LAS const char* p1) {
    s16x4 a = __builtin_bit_cast(s16x4, __builtin_amdgcn_ds_read_tr16_b64_v4i16((LAS s16x4*)p0));
    s16x4 b = __builtin_bit_cast(s16x4, __builtin_amdgcn_ds_read_tr16_b64_v4i16((LAS s16x4*)p1));
    return __builtin_bit_cast(bf16x8, __builtin_shufflevector(a, b, 0, 1, 2, 3, 4, 5, 6, 7));
}
DI u32x4 pack_lo(const f32x16& c) { return U4(__builtin_convertvector(__builtin_shufflevector(c, c, 0, 1, 2, 3, 4, 5, 6, 7), bf16x8)); }
DI u32x4 pack_hi(const f32x16& c) { return U4(__builtin_convertvector(__builtin_shufflevector(c, c, 8, 9, 10, 11, 12, 13, 14, 15), bf16x8)); }
DI unsigned cvt2(float a, float b) { return __builtin_bit_cast(unsigned, __builtin_convertvector((f32x2){a, b}, bf16x2)); }
DI float swap32(float v, bool hi) {
    auto r = __builtin_amdgcn_permlane32_swap(__float_as_uint(v), __float_as_uint(v), false, false);
    return hi ? __uint_as_float(r[0]) : __uint_as_float(r[1]);
}
DI u32x4 piece(u32x4 v) {
    auto a = __builtin_amdgcn_permlane32_swap(v[0], v[2], false, false), b = __builtin_amdgcn_permlane32_swap(v[1], v[3], false, false);
    return (u32x4){a[0], b[0], a[1], b[1]};
}
DI float sumsq(u32x4 v) {
    float s = 0.f;
#pragma unroll
    for (int i = 0; i < 4; ++i) { const unsigned u = v[i]; const bf16x2 p = __builtin_bit_cast(bf16x2, u); s = __builtin_amdgcn_fdot2_f32_bf16(p, p, s, false); }
    return s;
}
DI u64 poll64(GAS u64* p) { u64 z = 0; asm volatile("" : "+v"(z)); return __hip_atomic_fetch_add(p, z, RLX_AGENT); }
DI unsigned pos_bits(float v) { return (v == v) ? __float_as_uint(v) : 0x7F800000u; }
DI void dma16(u32x4 rsrc, unsigned voff, unsigned soff, unsigned lds_dst) {
    unsigned keep;
    asm volatile("s_mov_b32 %0, m0\n\ts_mov_b32 m0, %4\n\ts_nop 0\n\tbuffer_load_dwordx4 %1, %2, %3 offen lds\n\ts_mov_b32 m0, %0"
                 : "=&s"(keep) : "v"(voff), "s"(rsrc), "s"(soff), "s"(lds_dst) : "memory");
}
DI u32x4 mk_rsrc(const void* p, unsigned bytes) { const u64 a = (u64)p; return (u32x4){(unsigned)a, (unsigned)(a >> 32) & 0xffffu, bytes, 0x00020000u}; }
#define WAITV(n) asm volatile("s_waitcnt vmcnt(%c0)" :: "i"(n) : "memory")
#define BAR() asm volatile("s_waitcnt lgkmcnt(0)\n\ts_barrier" ::: "memory")
DI void lds_max(unsigned addr, unsigned v) { asm volatile("ds_max_u32 %0, %1" :: "v"(addr), "v"(v) : "memory"); }
DI void wait4(LAS unsigned* cnt) { while (__builtin_amdgcn_readfirstlane(__hip_atomic_load(cnt, RLX_WG)) < 4u) __builtin_amdgcn_s_sleep(1); MEMFENCE(); }
DI void sync4(LAS unsigned* cnt, int lane) {
    asm volatile("s_waitcnt lgkmcnt(0)" ::: "memory");
    if (lane == 0) __hip_atomic_fetch_add(cnt, 1u, RLX_WG);
    while (__builtin_amdgcn_readfirstlane(__hip_atomic_load(cnt, RLX_WG)) < 4u) __builtin_amdgcn_s_sleep(1);
    MEMFENCE();
}

__global__ __launch_bounds__(512, 2) void k_mha(const __bf16* __restrict__ x, const __bf16* __restrict__ w, const __bf16* __restrict__ wo, char* ws, __bf16* __restrict__ out) {
    extern __shared__ __attribute__((aligned(16))) char lds_[];
    asm volatile("" :: "s"(x), "s"(w), "s"(wo), "s"(ws), "s"(out));
    LAS char* lds = (LAS char*)lds_;
    LAS unsigned* misc = (LAS unsigned*)(lds + LDS_MISC);
    const unsigned lds0 = (unsigned)(uintptr_t)lds;
    const int bid = blockIdx.x;
    const int h = (bid & 1) * 8 + ((bid >> 3) & 7), r = ((bid >> 1) & 3) * 4 + (bid >> 6);
    const int lane = threadIdx.x & 63, wid = __builtin_amdgcn_readfirstlane(threadIdx.x >> 6), w4 = wid & 3;
    const int c = lane & 31; const bool hh = lane >= 32;
    const int T = 4 * r + w4;
    const u64 t_entry = __builtin_amdgcn_s_memrealtime();
    const __amdgpu_buffer_rsrc_t rws = __builtin_amdgcn_make_buffer_rsrc(ws, 0, WS_END, 0x00020000);
    const int blk = (lane >> 4) & 1, q4 = (lane & 15) >> 2, p4 = lane & 3, h8 = hh ? 8 : 0;
    const unsigned xa0 = LDS_X + w4 * 4096 + c * 128 + (((hh ? 1 : 0) ^ ((c >> 1) & 7)) * 16);

    if (wid >= 4) {
        asm volatile("; role B" ::: "memory");
        __builtin_amdgcn_s_waitcnt(0x0F70);
        const u32x4 rw = mk_rsrc(w, DM * N3 * 2), rwt = mk_rsrc(w, h < 14 ? DM * N3 * 2 : 0);
        const unsigned vw = (lane >> 3) * (N3 * 2) + 896 + (h < 14 ? h : 0) * 128 + (((lane & 7) ^ (((lane >> 4) & 1) << 2)) * 16);
#define LDW(p) do { dma16(rwt, vw, (8 * (p) + w4) * 8 * N3 * 2, lds0 + LDS_W + (8 * (p) + w4) * 1024); dma16(rwt, vw, (8 * (p) + 4 + w4) * 8 * N3 * 2, lds0 + LDS_W + (8 * (p) + 4 + w4) * 1024); } while (0)
        LDW(0); LDW(1); LDW(2);
        STAMPV(0, t_entry); STAMP(1);
        const unsigned vq = ((lane >> 2) * N3 + ((7 * h) >> 1) * 8 + (lane & 3) * 8) * 2;
        bf16x8 xf[28];
#define RDX(p) do { _Pragma("unroll") for (int s = 0; s < 4; ++s) xf[4 * (p) + s] = *(LAS const bf16x8*)(lds + ((p) % 6) * XSLOT + (xa0 ^ (32 * s))); } while (0)
#define LDQ(p) dma16(rw, vq, ((p) * 64 + 16 * w4) * N3 * 2, lds0 + LDS_WQ + (4 * (p) + w4) * 1024)
        WAITV(4 + NST); BAR(); LDW(3); RDX(0);
        WAITV(4 + NST); BAR(); LDW(4); RDX(1);
        WAITV(4 + NST); BAR(); LDW(5); RDX(2);
        WAITV(4); BAR(); LDW(6); LDQ(0); LDQ(1); LDQ(2); RDX(3);
        WAITV(7); BAR(); LDQ(3); LDQ(4); RDX(4);
        WAITV(7); BAR(); LDQ(5); LDQ(6); RDX(5);
        WAITV(7); BAR(); RDX(6);
        STAMP(4);
        WAITV(0); sync4(misc + 3, lane);
        STAMP(5);
        {
            LAS const char* qb = lds + LDS_WQ + (h8 + q4) * 64 + 32 * blk + 8 * p4;
            f32x16 aq = {};
            bf16x8 wq[28];
#pragma unroll
            for (int s = 0; s < 6; ++s) wq[s] = tr_frag(qb + 1024 * s, qb + 1024 * s + 256);
            SB();
#pragma unroll
            for (int s = 0; s < 28; ++s) { aq = MFMA32(wq[s], xf[s], aq); SB(); if (s + 6 < 28) wq[s + 6] = tr_frag(qb + 1024 * (s + 6), qb + 1024 * (s + 6) + 256); SB(); }
#pragma unroll
            for (int i = 0; i < 16; ++i) aq[i] *= QSCALE;
#pragma unroll
            for (int i = 0; i < 4; ++i) { aq[12 + i] = (hh && !(h & 1)) ? 0.f : aq[12 + i]; aq[i] = (!hh && (h & 1)) ? 0.f : aq[i]; }
            *(LAS u32x4*)(lds + LDS_Q + w4 * 2048 + lane * 16) = piece(pack_lo(aq));
            *(LAS u32x4*)(lds + LDS_Q + w4 * 2048 + 1024 + lane * 16) = piece(pack_hi(aq));
            float qn = 0.f;
#pragma unroll
            for (int i = 0; i < 16; ++i) qn = fmaf(aq[i], aq[i], qn);
            qn += swap32(qn, hh);
            lds_max(lds0 + LDS_MISC + 8, pos_bits(qn));
        }
        STAMP(6);
        sync4(misc + 7, lane);
        if (h >= 14) __builtin_amdgcn_endpgm();
        const int c3 = h >= 7 ? h - 7 : h, kh = w4 >> 1, T3 = 4 * r + (h >= 7 ? 2 : 0) + (w4 & 1);
        wait4(misc);
        STAMP(8);
        {
            const u32x4 rwo = mk_rsrc(wo, DM * DM * 2);
            const unsigned vo = (lane >> 3) * (DM * 2) + c3 * 128 + (((lane & 7) ^ (((lane >> 4) & 1) << 2)) * 16);
#pragma unroll
            for (int i = 0; i < 14; ++i) dma16(rwo, vo, (w4 + 4 * i) * 8 * DM * 2, lds0 + LDS_WO + (w4 + 4 * i) * 1024);
        }
        const u64 tag = (u64)misc[5] | ((u64)misc[6] << 32);
        const int sw = (q4 >> 1) & 1;
        unsigned b1 = LDS_WO + (kh * 8 * DH + (hh ? 4 : 0) + q4) * 128 + 64 * (sw ^ kh) + 32 * blk + 8 * p4;
        unsigned b3 = b1 + (hh ? 20 : 24) * 128;
        unsigned oin = WS_O + (T3 * 16 + 8 * kh) * 2048 + lane * 16;
        GAS u64* fp = (GAS u64*)(ws + WS_OFLAG + (T3 * 16 + 8 * kh + (lane & 7)) * 8);
        LAS char* exs = lds + LDS_XY + ((w4 & 1) * 2 + (kh ^ 1)) * 4096 + lane * 16;
        LAS const char* exr = lds + LDS_XY + ((w4 & 1) * 2 + kh) * 4096 + lane * 16;
        GAS __bf16* orow = (GAS __bf16*)out + (size_t)(32 * T3 + c) * DM + c3 * 64 + kh * 32 + h8;
        PIN(orow); PIN(fp); PIN(oin); PIN(b1); PIN(b3); PIN(exs); PIN(exr);
        if (bid == 0 && w4 == 0) {
            GAS u64* sp = (GAS u64*)(ws + WS_FLAG + (((lane >> 3) & 3) * 256 + 14) * 32 + (lane & 7) * 8);
            for (unsigned spins = 0;; ++spins) { if (__all((__hip_atomic_load(sp, RLX_AGENT) >> 16) == tag) || spins > SPIN_CAP) break; __builtin_amdgcn_s_sleep(8); }
        }
        f32x16 pa[2] = {};
        bf16x8 wf[8][2][2];
        STAMP(9);
        WAITV(0); BAR();
        STAMP(10);
        {
#pragma unroll
            for (int i = 0; i < 8; ++i)
#pragma unroll
                for (int a = 0; a < 2; ++a) {
                    LAS const char *p1 = lds + (b1 ^ (64 * a)), *p3 = lds + (b3 ^ (64 * a));
                    wf[i][0][a] = tr_frag(p1 + i * 3584, p1 + i * 3584 + 1024); wf[i][1][a] = tr_frag(p1 + i * 3584 + 2048, p3 + i * 3584);
                    PIN(wf[i][0][a]); PIN(wf[i][1][a]); }
            u64 fv = tag;
            for (unsigned spins = 0;; ++spins) {
                if (lane < 8) fv = poll64(fp);
                if (__all(fv == tag) || spins > SPIN_CAP) break;
            }
            STAMP(11);
            for (unsigned spins = 0;; ++spins) {
                MEMFENCE();
                u32x4 of[8][2];
#pragma unroll
                for (int i = 0; i < 8; ++i) { of[i][0] = __builtin_amdgcn_raw_buffer_load_b128(rws, oin, i * 2048, SC1); SB(); of[i][1] = __builtin_amdgcn_raw_buffer_load_b128(rws, oin + 1024, i * 2048, SC1); SB(); }
                unsigned mx = 0;
                pa[0] = (f32x16){}; pa[1] = (f32x16){};
#pragma unroll
                for (int i = 0; i < 8; ++i)
#pragma unroll
                    for (int k = 0; k < 2; ++k) {
                        pa[1] = MFMA32(wf[i][k][1], BF(of[i][k]), pa[1]); SB();
                        mx = max(max(mx, of[i][k][0]), of[i][k][1]); mx = max(max(mx, of[i][k][2]), of[i][k][3]); PIN(mx); SB();
                        pa[0] = MFMA32(wf[i][k][0], BF(of[i][k]), pa[0]); SB();
                    }
                if (!__any(mx == SENT) || spins > SPIN_CAP) break;
            }
        }
        STAMP(12);
        {
#pragma unroll
            for (int g = 0; g < 4; ++g) *(LAS f32x4*)(exs + g * 1024) = (f32x4){pa[1][4 * g], pa[1][4 * g + 1], pa[1][4 * g + 2], pa[1][4 * g + 3]};
            BAR();
#pragma unroll
            for (int g = 0; g < 4; ++g) { const f32x4 v = *(LAS const f32x4*)(exr + g * 1024);
#pragma unroll
                for (int i = 0; i < 4; ++i) pa[0][4 * g + i] += v[i]; }
            const __amdgpu_buffer_rsrc_t rout = __builtin_amdgcn_make_buffer_rsrc(out, 0, SEQ * DM * 2, 0x00020000);
            const unsigned oo = ((32 * T3 + c) * DM + c3 * 64 + kh * 32 + h8) * 2;
            __builtin_amdgcn_raw_buffer_store_b128(piece(pack_lo(pa[0])), rout, oo, 0, 16);
            __builtin_amdgcn_raw_buffer_store_b128(piece(pack_hi(pa[0])), rout, oo + 32, 0, 16);
        }
        if (bid == 0 && threadIdx.x == 256) __hip_atomic_store((GAS u64*)(ws + WS_EPOCH), tag, RLX_AGENT);
        STAMP(13);
        __builtin_amdgcn_endpgm();
    } else {
        const u32x4 rx = mk_rsrc(x, SEQ * DM * 2);
        const unsigned vx = (lane >> 3) * 896 + (((lane & 7) ^ (lane >> 4)) * 16), vx1 = vx ^ 64;
        const unsigned xs0 = 32 * T * 896;
#define LDX(p) do { const unsigned sl = lds0 + LDS_X + ((p) % 6) * XSLOT + w4 * 4096; \
            dma16(rx, vx, xs0 + (p) * 128, sl); dma16(rx, vx1, xs0 + (p) * 128 + 8 * 896, sl + 1024); dma16(rx, vx, xs0 + (p) * 128 + 16 * 896, sl + 2048); dma16(rx, vx1, xs0 + (p) * 128 + 24 * 896, sl + 3072); } while (0)
        LDX(0); LDX(1); LDX(2);
        SB();
        u64 epoch;
        asm volatile("global_load_dwordx2 %0, %1, %2 sc1" : "=v"(epoch) : "v"(WS_EPOCH), "s"(ws) : "memory");
#define NPT 0
        if (threadIdx.x < 12) misc[threadIdx.x] = 0u;
        const unsigned ooff = WS_O + (T * 16 + h) * 2048 + lane * 16;
        __builtin_amdgcn_raw_buffer_store_b128((u32x4){SENT, SENT, SENT, SENT}, rws, ooff, 0, SC1);
        __builtin_amdgcn_raw_buffer_store_b128((u32x4){SENT, SENT, SENT, SENT}, rws, ooff + 1024, 0, SC1);
        SB();
        const bool isk = h < 7, isv = h >= 7 && h < 14;
        const int ct = isk ? h : h - 7;
        unsigned dst0, dst1, dstp;
        { const int c0 = 64 * ct + c, h0 = (c0 * 293) >> 13, c1 = c0 + 32, h1 = (c1 * 293) >> 13;
          dst0 = WS_KV + T * TREC + (isk ? ct * 4096 + lane * 16 : TR_V + h0 * 2048 + (c0 - DH * h0 + (hh ? 32 : 0)) * 16);
          dst1 = WS_KV + T * TREC + TR_V + h1 * 2048 + (c1 - DH * h1 + (hh ? 32 : 0)) * 16;
          const int hp = (64 * ct) / DH + (lane >> 4);
          dstp = (DH * hp + DH - 1 < 64 * ct + 64) ? WS_KV + T * TREC + TR_V + hp * 2048 + ((lane >> 2) & 3) * 512 + (DH + (lane & 3)) * 16 : OOB;
          PIN(dst0); PIN(dst1); PIN(dstp); }
        const int sw = (q4 >> 1) & 1;
        const unsigned wa0 = LDS_W + (h8 + q4) * 128 + 64 * sw + 32 * blk + 8 * p4, wa1 = wa0 ^ 64;
        f32x16 a0 = {}, a1 = {};
        bf16x8 xf_[2][4], w0_[2][4], w1_[2][4];
        STAMPV(0, t_entry); STAMP(1);
#define XW(p) ((p) <= 2 ? 11 + NPT + NST : (p) <= 4 ? 8 : 4 * (6 - (p)))
#define P1RD1(p, s) do { xf_[(p) & 1][s] = *(LAS const bf16x8*)(lds + ((p) % 6) * XSLOT + (xa0 ^ (32 * (s)))); \
            w0_[(p) & 1][s] = tr_frag(lds + wa0 + 8192 * (p) + 2048 * (s), lds + wa0 + 8192 * (p) + 2048 * (s) + 512); } while (0)
#define P1RD2(p, s) do { w1_[(p) & 1][s] = tr_frag(lds + wa1 + 8192 * (p) + 2048 * (s), lds + wa1 + 8192 * (p) + 2048 * (s) + 512); } while (0)
#define P1M(acc, wv, p, s, KT) do { if (KT) acc = MFMA32(wv[(p) & 1][s], xf_[(p) & 1][s], acc); else acc = MFMA32(xf_[(p) & 1][s], wv[(p) & 1][s], acc); } while (0)
#define P1X(p, s) (xf_[(p) & 1][s] = *(LAS const bf16x8*)(lds + ((p) % 6) * XSLOT + (xa0 ^ (32 * (s)))))
#define P1W(wv, wa, p, s) (wv[(p) & 1][s] = tr_frag(lds + wa + 8192 * (p) + 2048 * (s), lds + wa + 8192 * (p) + 2048 * (s) + 512))
#define P1MM(acc, wv, p, s, KT) do { if ((p) > 0) P1M(acc, wv, (p) - 1, s, KT); SB(); } while (0)
#define P1STEP(p, KT) do { WAITV(XW(p)); BAR(); SB(); if (KT < 2) { \
            P1MM(a0, w0_, p, 0, KT); P1X(p, 0); P1W(w0_, wa0, p, 0); SB(); P1MM(a1, w1_, p, 0, KT); P1W(w1_, wa1, p, 0); P1X(p, 1); SB(); \
            P1MM(a0, w0_, p, 1, KT); P1W(w0_, wa0, p, 1); P1W(w1_, wa1, p, 1); SB(); P1MM(a1, w1_, p, 1, KT); P1X(p, 2); P1W(w0_, wa0, p, 2); SB(); \
            P1MM(a0, w0_, p, 2, KT); P1W(w1_, wa1, p, 2); P1X(p, 3); SB(); P1MM(a1, w1_, p, 2, KT); P1W(w0_, wa0, p, 3); P1W(w1_, wa1, p, 3); SB(); \
            P1MM(a0, w0_, p, 3, KT); } if ((p) <= 3) LDX((p) + 3); SB(); if (KT < 2) P1MM(a1, w1_, p, 3, KT); } while (0)
#define P1LAST(KT) do { _Pragma("unroll") for (int s = 0; s < 4; ++s) { P1M(a0, w0_, 6, s, KT); P1M(a1, w1_, 6, s, KT); } SB(); } while (0)
#define P1ALL(KT) do { P1STEP(0, KT); P1STEP(1, KT); P1STEP(2, KT); P1STEP(3, KT); P1STEP(4, KT); P1STEP(5, KT); P1STEP(6, KT); } while (0)
        float stat = 0.f;
        if (isk) {
            P1ALL(1); STAMP(3); P1LAST(1);
            STAMP(4);
            const u32x4 k0 = piece(pack_lo(a0)), k1 = piece(pack_hi(a0)), k2 = piece(pack_lo(a1)), k3 = piece(pack_hi(a1));
            __builtin_amdgcn_raw_buffer_store_b128(k0, rws, dst0, 0, SC1); __builtin_amdgcn_raw_buffer_store_b128(k1, rws, dst0 + 1024, 0, SC1);
            __builtin_amdgcn_raw_buffer_store_b128(k2, rws, dst0 + 2048, 0, SC1); __builtin_amdgcn_raw_buffer_store_b128(k3, rws, dst0 + 3072, 0, SC1);
            SB();
            stat = (sumsq(k0) + sumsq(k1)) + (sumsq(k2) + sumsq(k3));
            stat += swap32(stat, hh);
        } else if (isv) {
            P1ALL(0); STAMP(3); P1LAST(0);
            STAMP(4);
            const u32x4 k0 = pack_lo(a0), k1 = pack_hi(a0), k2 = pack_lo(a1), k3 = pack_hi(a1);
            __builtin_amdgcn_raw_buffer_store_b128(k0, rws, dst0, 0, SC1); __builtin_amdgcn_raw_buffer_store_b128(k1, rws, dst0 + 1024, 0, SC1);
            __builtin_amdgcn_raw_buffer_store_b128(k2, rws, dst1, 0, SC1); __builtin_amdgcn_raw_buffer_store_b128(k3, rws, dst1 + 1024, 0, SC1);
            const unsigned one = (lane & 3) == 0 ? 0x3F803F80u : 0u;
            __builtin_amdgcn_raw_buffer_store_b128((u32x4){one, one, one, one}, rws, dstp, 0, SC1);
            SB();
            stat = (sumsq(k0) + sumsq(k1)) + (sumsq(k2) + sumsq(k3));
        } else {
            P1ALL(2); STAMP(3); STAMP(4);
        }
        lds_max(lds0 + LDS_MISC + 4, pos_bits(stat));
        asm volatile("" : "+v"(epoch));
        const u64 tag = (((u64)__builtin_amdgcn_readfirstlane((unsigned)(epoch >> 32)) << 32 | (unsigned)__builtin_amdgcn_readfirstlane((unsigned)epoch)) + 1ull) & 0xFFFFFFFFFFFFull;
        if (threadIdx.x == 0) { misc[5] = (unsigned)tag; misc[6] = (unsigned)(tag >> 32); }
        GAS u64* fo = (GAS u64*)(ws + WS_FLAG + (((r >> 2) * 16 + (lane & 15)) * 16 + h) * 32 + (r & 3) * 8);
        PIN(fo);
        STAMP(5);
        asm volatile("s_waitcnt vmcnt(0) lgkmcnt(0)" ::: "memory");
        {
            unsigned arr = 0;
            if (lane == 0) arr = __hip_atomic_fetch_add(misc, 1u, RLX_WG);
            if (__builtin_amdgcn_readfirstlane(arr) == 3u && lane < 16) {
                const unsigned sb = (__hip_atomic_load(misc + 1, RLX_WG) + 0xFFFFu) >> 16;
                (void)__hip_atomic_exchange(fo, tag << 16 | sb, RLX_AGENT);
            }
        }
        STAMP(6);
        const int pc0 = (7 * h) >> 1, ka = pc0 >> 3, kb = (pc0 + 3) >> 3, va = (DH * h) >> 6, vb = (DH * h + DH - 1) >> 6;
        const int l4 = (lane >> 2) & 3;
        GAS u64* fp = (GAS u64*)(ws + WS_FLAG + ((w4 * 16 + r) * 16 + (l4 == 0 ? ka : l4 == 1 ? kb : l4 == 2 ? 7 + va : 7 + vb)) * 32 + (lane & 3) * 8);
        u64 fv = tag << 16;
        if (lane < 16) fv = poll64(fp);
        SB();
        f32x16 o[4] = {};
        PIN(o[0]); PIN(o[1]); PIN(o[2]); PIN(o[3]);
        wait4(misc + 7);
        bf16x8 qf[4][2];
#pragma unroll
        for (int t = 0; t < 4; ++t) { const int tg = (w4 + 1 + t) & 3; qf[t][0] = *(LAS const bf16x8*)(lds + LDS_Q + tg * 2048 + lane * 16); qf[t][1] = *(LAS const bf16x8*)(lds + LDS_Q + tg * 2048 + 1024 + lane * 16); }
        const float qn = __uint_as_float(misc[2]);
        STAMP(8);
        LAS char* cmb[3];
#pragma unroll
        for (int t = 0; t < 3; ++t) cmb[t] = lds + cmb_slot(((w4 + 1 + t) & 3) * 3 + 2 - t) + lane * 16;
#define SENDQ(t, g) (*(LAS f32x4*)(cmb[t] + (g) * 1024) = (f32x4){o[t][4 * (g)], o[t][4 * (g) + 1], o[t][4 * (g) + 2], o[t][4 * (g) + 3]})
#define SEND(t, mv) do { o[t][13] = hh ? (mv) : o[t][13]; SENDQ(t, 0); SENDQ(t, 1); SENDQ(t, 2); SENDQ(t, 3); } while (0)
        for (unsigned spins = 0; !__all((fv >> 16) == tag) && spins <= SPIN_CAP; ++spins) { if (lane < 16) fv = poll64(fp); }
        MEMFENCE();
        STAMP(7);
        const __amdgpu_buffer_rsrc_t rk = __builtin_amdgcn_make_buffer_rsrc(ws + WS_KV + 16 * w4 * TREC + pc0 * 512, 16, 16 * TREC, 1 << 23);
        const __amdgpu_buffer_rsrc_t rv = __builtin_amdgcn_make_buffer_rsrc(ws + WS_KV + 16 * w4 * TREC + TR_V + h * 2048, 16, 16 * TREC, 1 << 23);
#define LDF(rs_, f_, so_) __builtin_amdgcn_raw_buffer_load_b128(rs_, (f_) * 1024, so_, SC1)
        u32x4 kf[4][2], vf[4][2];
#pragma unroll
        for (int b = 0; b < 4; ++b) { kf[b][0] = LDF(rk, 0, b * TREC); SB(); kf[b][1] = LDF(rk, 1, b * TREC); SB();
            if (b < 3) { vf[b][0] = LDF(rv, 0, b * TREC); SB(); vf[b][1] = LDF(rv, 1, b * TREC); SB(); } }
        vf[3][0] = (u32x4){0u, 0u, 0u, 0u}; vf[3][1] = vf[3][0];
        SB();
        unsigned sa_ = 0, sb_ = 0, sv_ = 0;
        { const unsigned s16 = (unsigned)fv << 16;
#pragma unroll
          for (int i = 0; i < 4; ++i) { sa_ = max(sa_, (unsigned)__builtin_amdgcn_readlane(s16, i)); sb_ = max(sb_, (unsigned)__builtin_amdgcn_readlane(s16, 4 + i));
              sv_ = max(sv_, max((unsigned)__builtin_amdgcn_readlane(s16, 8 + i), (unsigned)__builtin_amdgcn_readlane(s16, 12 + i))); } }
        const float kn = __uint_as_float(sa_) + (ka != kb ? __uint_as_float(sb_) : 0.f);
        int fasti = (kn * qn * 1.05f <= SAFE_T2 && __uint_as_float(sv_) <= SAFE_V2) ? 1 : 0;
        const bool fast = __builtin_amdgcn_readfirstlane(fasti) != 0;
        float m[4] = {0.f, 0.f, 0.f, 0.f};
        STAMP(9); STAMPC(14);
        if (__builtin_expect(fast, 1)) {
            f32x16 sa, sb = {};
            u32x4 plo = {}, phi = {};
            const f32x16 zero = {};
            sa = MFMA32(BF(kf[0][0]), qf[0][0], zero); sa = MFMA32(BF(kf[0][1]), qf[0][1], sa);
            SB();
#define EX(S, i) S[i] = __builtin_amdgcn_exp2f(S[i])
#define STEP(SC, SN, B, t, X1, X2, X3, X4) do { \
            constexpr int tn = ((t) + 1) & 3, bn = (t) == 3 ? ((B) + 1) & 3 : (B), tp = ((t) + 3) & 3, bp = (t) == 0 ? ((B) + 3) & 3 : (B); \
            o[tp] = MFMA32(BF(vf[bp][0]), BF(plo), o[tp]); SB(); X1; SB(); \
            phi[2] = cvt2(SN[12], SN[13]); phi[3] = cvt2(SN[14], SN[15]); PIN(phi); SB(); EX(SC, 0); EX(SC, 1); EX(SC, 2); EX(SC, 3); PIN(SC); SB(); \
            SN = MFMA32(BF(kf[bn][0]), qf[tn][0], zero); SB(); X2; SB(); \
            plo[0] = cvt2(SC[0], SC[1]); plo[1] = cvt2(SC[2], SC[3]); PIN(plo); SB(); EX(SC, 4); EX(SC, 5); EX(SC, 6); EX(SC, 7); PIN(SC); SB(); \
            SN = MFMA32(BF(kf[bn][1]), qf[tn][1], SN); SB(); X3; SB(); \
            plo[2] = cvt2(SC[4], SC[5]); plo[3] = cvt2(SC[6], SC[7]); PIN(plo); SB(); EX(SC, 8); EX(SC, 9); EX(SC, 10); EX(SC, 11); PIN(SC); SB(); \
            o[tp] = MFMA32(BF(vf[bp][1]), BF(phi), o[tp]); SB(); X4; SB(); \
            phi[0] = cvt2(SC[8], SC[9]); phi[1] = cvt2(SC[10], SC[11]); PIN(phi); SB(); EX(SC, 12); EX(SC, 13); EX(SC, 14); EX(SC, 15); PIN(SC); SB(); \
            } while (0)
#define NOP (void)0
#define PIN4(a, b, c_, d) asm volatile("" : "+v"(a), "+v"(b), "+v"(c_), "+v"(d))
#define PINS(x) asm volatile("" : "+s"(x))
#define BLOCK(B) \
            STEP(sa, sb, B, 0, vf[((B) + 3) & 3][0] = LDF(rv, 0, so), PIN4(vf[B][0], vf[B][1], kf[((B) + 1) & 3][0], kf[((B) + 1) & 3][1]), NOP, vf[((B) + 3) & 3][1] = LDF(rv, 1, so)); \
            STEP(sb, sa, B, 1, do { so += TREC; PINS(so); } while (0), NOP, NOP, NOP); \
            STEP(sa, sb, B, 2, NOP, kf[B][0] = LDF(rk, 0, so), kf[B][1] = LDF(rk, 1, so), NOP); \
            STEP(sb, sa, B, 3, NOP, NOP, NOP, NOP)
            unsigned so = 3 * TREC;
            for (int it = 0; it < 3; ++it) { BLOCK(0); BLOCK(1); BLOCK(2); BLOCK(3); }
            STEP(sa, sb, 0, 0, vf[3][0] = LDF(rv, 0, so), PIN4(vf[0][0], vf[0][1], kf[1][0], kf[1][1]), NOP, vf[3][1] = LDF(rv, 1, so));
            STEP(sb, sa, 0, 1, NOP, NOP, NOP, NOP); STEP(sa, sb, 0, 2, NOP, NOP, NOP, NOP); STEP(sb, sa, 0, 3, NOP, NOP, NOP, NOP);
            STEP(sa, sb, 1, 0, NOP, PIN4(vf[1][0], vf[1][1], kf[2][0], kf[2][1]), NOP, NOP); STEP(sb, sa, 1, 1, NOP, NOP, NOP, NOP); STEP(sa, sb, 1, 2, NOP, NOP, NOP, NOP); STEP(sb, sa, 1, 3, NOP, NOP, NOP, NOP);
            STEP(sa, sb, 2, 0, NOP, PIN4(vf[2][0], vf[2][1], kf[3][0], kf[3][1]), NOP, NOP); STEP(sb, sa, 2, 1, NOP, NOP, NOP, NOP); STEP(sa, sb, 2, 2, NOP, NOP, NOP, NOP); STEP(sb, sa, 2, 3, NOP, NOP, NOP, NOP);
            STEP(sa, sb, 3, 0, NOP, PIN4(vf[3][0], vf[3][1], kf[3][0], kf[3][1]), NOP, NOP); STEP(sb, sa, 3, 1, NOP, NOP, NOP, NOP);
            STEP(sa, sb, 3, 2, NOP, SENDQ(0, 0), SENDQ(0, 1), SENDQ(0, 2));
            STEP(sb, sa, 3, 3, SENDQ(0, 3), SENDQ(1, 0), SENDQ(1, 1), SENDQ(1, 2));
            o[3] = MFMA32(BF(vf[3][0]), BF(plo), o[3]); SB(); SENDQ(1, 3); SB();
            phi[2] = cvt2(sb[12], sb[13]); phi[3] = cvt2(sb[14], sb[15]); SB();
            SENDQ(2, 0); SB();
            o[3] = MFMA32(BF(vf[3][1]), BF(phi), o[3]); SB();
            SENDQ(2, 1); SENDQ(2, 2); SENDQ(2, 3);
        }
        PIN(fasti);
        if (__builtin_expect(__builtin_amdgcn_readfirstlane(fasti) == 0, 0)) {
#pragma unroll
            for (int t = 0; t < 4; ++t) m[t] = -INFINITY;
            for (int j = 0; j < 16; ++j) {
                kf[0][0] = LDF(rk, 0, j * TREC); kf[0][1] = LDF(rk, 1, j * TREC); vf[0][0] = LDF(rv, 0, j * TREC); vf[0][1] = LDF(rv, 1, j * TREC);
#pragma unroll
                for (int t = 0; t < 4; ++t) {
                    SB();
                    f32x16 s = {};
                    s = MFMA32(BF(kf[0][0]), qf[t][0], s);
                    s = MFMA32(BF(kf[0][1]), qf[t][1], s);
                    float bm = s[0];
#pragma unroll
                    for (int i = 1; i < 16; ++i) bm = fmaxf(bm, s[i]);
                    bm = fmaxf(bm, swap32(bm, hh));
                    const float mn = fmaxf(m[t], bm), al = __builtin_amdgcn_exp2f(m[t] - mn);
                    m[t] = mn;
                    o[t] *= al; PIN(o[t]); SB();
#pragma unroll
                    for (int i = 0; i < 16; ++i) s[i] = __builtin_amdgcn_exp2f(s[i] - mn);
                    PIN(s); SB();
                    o[t] = MFMA32(BF(vf[0][0]), BF(pack_lo(s)), o[t]);
                    o[t] = MFMA32(BF(vf[0][1]), BF(pack_hi(s)), o[t]);
                    PIN(o[t]);
                }
            }
            SEND(0, m[0]); SEND(1, m[1]); SEND(2, m[2]);
            o[3][13] = hh ? m[3] : o[3][13];
        }
        STAMPC(15); STAMP(10);
        f32x16 acc = o[3];
        BAR();
        f32x16 p[3];
#pragma unroll
        for (int d = 0; d < 3; ++d)
#pragma unroll
            for (int g = 0; g < 4; ++g) { const f32x4 v = *(LAS const f32x4*)(lds + cmb_slot(w4 * 3 + d) + g * 1024 + lane * 16);
#pragma unroll
                for (int i = 0; i < 4; ++i) p[d][4 * g + i] = v[i]; }
        const bool plain = __all(!hh || (acc[13] == 0.f && p[0][13] == 0.f && p[1][13] == 0.f && p[2][13] == 0.f));
        if (__builtin_expect(plain, 1)) {
            acc = (acc + p[0]) + (p[1] + p[2]);
        } else {
            float mm[4] = {acc[13], p[0][13], p[1][13], p[2][13]};
#pragma unroll
            for (int d = 0; d < 4; ++d) { const float s = swap32(mm[d], hh); mm[d] = hh ? mm[d] : s; }
            const float M = fmaxf(fmaxf(mm[0], mm[1]), fmaxf(mm[2], mm[3]));
            acc *= __builtin_amdgcn_exp2f(mm[0] - M);
#pragma unroll
            for (int d = 0; d < 3; ++d) acc += p[d] * __builtin_amdgcn_exp2f(mm[d + 1] - M);
        }
        const float lsw = swap32(acc[12], hh);
        const float inv = __builtin_amdgcn_rcpf(hh ? acc[12] : lsw);
        acc *= inv;
        u32x4 lo = pack_lo(acc), hi = pack_hi(acc);
        if (hh) { hi[2] = 0u; hi[3] = 0u; }
#pragma unroll
        for (int i = 0; i < 4; ++i) { lo[i] = min(lo[i], SENT - 1u); hi[i] = min(hi[i], SENT - 1u); }
        __builtin_amdgcn_raw_buffer_store_b128(lo, rws, ooff, 0, SC1);
        __builtin_amdgcn_raw_buffer_store_b128(hi, rws, ooff + 1024, 0, SC1);
        MEMFENCE();
        if (lane == 0) (void)__hip_atomic_exchange((GAS u64*)(ws + WS_OFLAG + (T * 16 + h) * 8), tag, RLX_AGENT);
        STAMP(11);
        __builtin_amdgcn_endpgm();
    }
}

extern "C" void kernel_launch(void* const* d_in, const int* in_sizes, int n_in, void* d_out, int out_size, void* d_ws, size_t ws_size, hipStream_t stream) {
    (void)hipFuncSetAttribute((const void*)k_mha, hipFuncAttributeMaxDynamicSharedMemorySize, LDS_BYTES);
    k_mha<<<256, 512, LDS_BYTES, stream>>>((const __bf16*)d_in[0], (const __bf16*)d_in[1], (const __bf16*)d_in[2], (char*)d_ws, (__bf16*)d_out);
}
```

```cpp
#include <hip/hip_runtime.h>
#include <hip/hip_bf16.h>
#include <stdint.h>

typedef __bf16 bf16x8 __attribute__((ext_vector_type(8)));
typedef __bf16 bf16x4 __attribute__((ext_vector_type(4)));
typedef __bf16 bf16x2 __attribute__((ext_vector_type(2)));
typedef float f32x16 __attribute__((ext_vector_type(16)));
typedef float f32x8 __attribute__((ext_vector_type(8)));
typedef float f32x4 __attribute__((ext_vector_type(4)));
typedef float f32x2 __attribute__((ext_vector_type(2)));
typedef unsigned u32x4 __attribute__((ext_vector_type(4)));
typedef unsigned u32x2 __attribute__((ext_vector_type(2)));
typedef short s16x4 __attribute__((ext_vector_type(4)));
typedef unsigned long long u64;
#define LAS __attribute__((address_space(3)))
#define GAS __attribute__((address_space(1)))
#define DI __device__ __forceinline__
#define DI_ __device__ __forceinline__
#define MFMA32(a, b, c) __builtin_amdgcn_mfma_f32_32x32x16_bf16((a), (b), (c), 0, 0, 0)
#define MFMA16(a, b, c) __builtin_amdgcn_mfma_f32_16x16x32_bf16((a), (b), (c), 0, 0, 0)
#define SB() __builtin_amdgcn_sched_barrier(0)
#define PIN(x) asm volatile("" : "+v"(x))
#define MEMFENCE() asm volatile("" ::: "memory")
#define RLX_AGENT __ATOMIC_RELAXED, __HIP_MEMORY_SCOPE_AGENT
#define RLX_WG __ATOMIC_RELAXED, __HIP_MEMORY_SCOPE_WORKGROUP
#define SC1 16
#define BF(x) __builtin_bit_cast(bf16x8, x)
#define U4(x) __builtin_bit_cast(u32x4, x)
#define TWO 1
#define WOLATE 1
#define HEAT 0
#define INCR 0
#define TAILV 0
#define XD 3

constexpr int SEQ = 2048, DM = 448, NH = 16, DH = 28, N3 = 1344;
constexpr float QSCALE = 0.18898223650461362f * 1.4426950408889634f;
constexpr unsigned TREC = 61440, TR_V = 28672;
constexpr unsigned WS_KV = 4096, WS_O = 4u << 20, WS_FLAG = 6u << 20, WS_OFLAG = WS_FLAG + 32768, WS_EPOCH = WS_OFLAG + 8192, WS_END = WS_EPOCH + 256;
constexpr int LDS_W = 0, LDS_X = 57344, XSLOT = 16384, LDS_WQ = LDS_X + XSLOT, LDS_WO = LDS_X, LDS_Q = 40960, LDS_MISC = 157696, LDS_XY = LDS_WO + 57344, LDS_BYTES = LDS_MISC + 64;
DI_ int cmb_slot(int k) { return 4096 * (k < 10 ? k : k + 2); }
constexpr float SAFE_T2 = 90.f * 90.f;
constexpr float SAFE_V2 = 1.0e12f;
constexpr unsigned SPIN_CAP = 1u << 18;
constexpr unsigned SENT = 0xFFFFFFFFu, OOB = 0xFFFFFF00u;

#define STAMP(i)
#define STAMPC(i)
#define STAMPV(i, v)
#define NST 0

DI bf16x8 tr_frag(LAS const char* p0, LAS const char* p1) {
    s16x4 a = __builtin_bit_cast(s16x4, __builtin_amdgcn_ds_read_tr16_b64_v4i16((LAS s16x4*)p0));
    s16x4 b = __builtin_bit_cast(s16x4, __builtin_amdgcn_ds_read_tr16_b64_v4i16((LAS s16x4*)p1));
    return __builtin_bit_cast(bf16x8, __builtin_shufflevector(a, b, 0, 1, 2, 3, 4, 5, 6, 7));
}
DI u32x4 pack_lo(const f32x16& c) { return U4(__builtin_convertvector(__builtin_shufflevector(c, c, 0, 1, 2, 3, 4, 5, 6, 7), bf16x8)); }
DI u32x4 pack_hi(const f32x16& c) { return U4(__builtin_convertvector(__builtin_shufflevector(c, c, 8, 9, 10, 11, 12, 13, 14, 15), bf16x8)); }
DI unsigned cvt2(float a, float b) { return __builtin_bit_cast(unsigned, __builtin_convertvector((f32x2){a, b}, bf16x2)); }
DI float swap32(float v, bool hi) {
    auto r = __builtin_amdgcn_permlane32_swap(__float_as_uint(v), __float_as_uint(v), false, false);
    return hi ? __uint_as_float(r[0]) : __uint_as_float(r[1]);
}
DI u32x4 piece(u32x4 v) {
    auto a = __builtin_amdgcn_permlane32_swap(v[0], v[2], false, false), b = __builtin_amdgcn_permlane32_swap(v[1], v[3], false, false);
    return (u32x4){a[0], b[0], a[1], b[1]};
}
DI float sumsq(u32x4 v) {
    float s = 0.f;
#pragma unroll
    for (int i = 0; i < 4; ++i) { const unsigned u = v[i]; const bf16x2 p = __builtin_bit_cast(bf16x2, u); s = __builtin_amdgcn_fdot2_f32_bf16(p, p, s, false); }
    return s;
}
DI u64 poll64(GAS u64* p) { u64 z = 0; asm volatile("" : "+v"(z)); return __hip_atomic_fetch_add(p, z, RLX_AGENT); }
#define WAIT_FLAGS(res, ta, tb, off, tag_, MASK, SH) do { u64 t_; unsigned n_; \
    asm volatile("s_mov_b64 exec, " MASK "\n\ts_mov_b32 %4, %8\n\t" \
                 "v_mov_b64 %1, 0\n\tglobal_atomic_add_x2 %1, %5, %1, %6 sc0\n\ts_sleep 6\n" \
                 "1:\n\tv_mov_b64 %2, 0\n\tglobal_atomic_add_x2 %2, %5, %2, %6 sc0\n\ts_waitcnt vmcnt(1)\n\t" \
                 "v_lshrrev_b64 %3, " SH ", %1\n\tv_mov_b64 %0, %1\n\tv_cmp_ne_u64 vcc, %7, %3\n\ts_cbranch_vccz 2f\n\t" \
                 "v_mov_b64 %1, 0\n\tglobal_atomic_add_x2 %1, %5, %1, %6 sc0\n\ts_waitcnt vmcnt(1)\n\t" \
                 "v_lshrrev_b64 %3, " SH ", %2\n\tv_mov_b64 %0, %2\n\tv_cmp_ne_u64 vcc, %7, %3\n\ts_cbranch_vccz 2f\n\t" \
                 "s_sub_u32 %4, %4, 1\n\ts_cbranch_scc0 1b\n" \
                 "2:\n\ts_mov_b64 exec, -1" \
                 : "=&v"(res), "=&v"(ta), "=&v"(tb), "=&v"(t_), "=&s"(n_) : "v"(off), "s"(ws), "s"(tag_), "i"(SPIN_CAP) : "memory", "vcc", "scc"); } while (0)
DI unsigned pos_bits(float v) { return (v == v) ? __float_as_uint(v) : 0x7F800000u; }
DI void dma16(u32x4 rsrc, unsigned voff, unsigned soff, unsigned lds_dst) {
    unsigned keep;
    asm volatile("s_mov_b32 %0, m0\n\ts_mov_b32 m0, %4\n\ts_nop 0\n\tbuffer_load_dwordx4 %1, %2, %3 offen lds\n\ts_mov_b32 m0, %0"
                 : "=&s"(keep) : "v"(voff), "s"(rsrc), "s"(soff), "s"(lds_dst) : "memory");
}
DI u32x4 mk_rsrc(const void* p, unsigned bytes) { const u64 a = (u64)p; return (u32x4){(unsigned)a, (unsigned)(a >> 32) & 0xffffu, bytes, 0x00020000u}; }
#define WAITV(n) asm volatile("s_waitcnt vmcnt(%c0)" :: "i"(n) : "memory")
#define BAR() asm volatile("s_waitcnt lgkmcnt(0)\n\ts_barrier" ::: "memory")
DI void lds_max(unsigned addr, unsigned v) { asm volatile("ds_max_u32 %0, %1" :: "v"(addr), "v"(v) : "memory"); }
DI void wait4(LAS unsigned* cnt) {
    for (unsigned n = 0; __builtin_amdgcn_readfirstlane(__hip_atomic_load(cnt, RLX_WG)) < 4u && n < (SPIN_CAP << 6); ++n) __builtin_amdgcn_s_sleep(1);
    MEMFENCE();
}
DI void sync4(LAS unsigned* cnt, int lane) {
    asm volatile("s_waitcnt lgkmcnt(0)" ::: "memory");
    if (lane == 0) __hip_atomic_fetch_add(cnt, 1u, RLX_WG);
    wait4(cnt);
}

__global__ __launch_bounds__(512, 2) void k_mha(const __bf16* x, const __bf16* w, const __bf16* wo, char* ws, __bf16* out) {
    extern __shared__ __attribute__((aligned(16))) char lds_[];
    LAS char* lds = (LAS char*)lds_;
    LAS unsigned* misc = (LAS unsigned*)(lds + LDS_MISC);
    const unsigned lds0 = (unsigned)(uintptr_t)lds;
    const int bid = blockIdx.x;
    const int h = (bid & 1) * 8 + ((bid >> 3) & 7), r = ((bid >> 1) & 3) * 4 + (bid >> 6);
    const int lane = threadIdx.x & 63, wid = __builtin_amdgcn_readfirstlane(threadIdx.x >> 6), w4 = wid & 3;
    const int c = lane & 31; const bool hh = lane >= 32;
    const int T = 4 * r + w4;
    const bool roleA = wid < 4;
    const unsigned vx = (lane >> 3) * 896 + (((lane & 7) ^ (lane >> 4)) * 16), vx1 = vx ^ 64;
    const unsigned vw = (lane >> 3) * (N3 * 2) + 896 + (h < 14 ? h : 0) * 128 + (((lane & 7) ^ (((lane >> 4) & 1) << 2)) * 16);
    const unsigned xs0 = 32 * T * 896;
    unsigned ve = roleA ? vx : vw, vo = roleA ? vx1 : vw;
    unsigned s0 = roleA ? xs0 : w4 * 8 * N3 * 2, ds = roleA ? 7168 : 32 * N3 * 2, l0 = lds0 + (roleA ? LDS_X + w4 * 4096 : LDS_W + w4 * 1024), dl = roleA ? 1024 : 4096;
    asm volatile("" : "+v"(ve), "+v"(vo), "+s"(s0), "+s"(ds), "+s"(l0), "+s"(dl));
    SB();
    asm volatile("" : "+s"(x), "+s"(w), "+s"(wo), "+s"(ws), "+s"(out));
    const u32x4 rx = mk_rsrc(x, SEQ * DM * 2), rwt = mk_rsrc(w, h < 14 ? DM * N3 * 2 : 0);
    {
        const u32x4 r0 = roleA ? rx : rwt;
#pragma unroll
        for (int k = 0; k < 4; ++k) dma16(r0, (k & 1) ? vo : ve, s0 + k * ds, l0 + k * dl);
    }
    SB();
    const __amdgpu_buffer_rsrc_t rws = __builtin_amdgcn_make_buffer_rsrc(ws, 0, WS_END, 0x00020000);
    const int blk = (lane >> 4) & 1, q4 = (lane & 15) >> 2, p4 = lane & 3, h8 = hh ? 8 : 0;
    const unsigned xa0 = LDS_X + w4 * 4096 + c * 128 + (((hh ? 1 : 0) ^ ((c >> 1) & 7)) * 16);

    if (!roleA) {
        asm volatile("; role B" ::: "memory");
        const u32x4 rw = mk_rsrc(w, DM * N3 * 2);
#define LDW(p) do { dma16(rwt, vw, (8 * (p) + w4) * 8 * N3 * 2, lds0 + LDS_W + (8 * (p) + w4) * 1024); dma16(rwt, vw, (8 * (p) + 4 + w4) * 8 * N3 * 2, lds0 + LDS_W + (8 * (p) + 4 + w4) * 1024); } while (0)
        LDW(2);
        STAMPV(0, t_entry); STAMP(1);
        const unsigned vq = ((lane >> 2) * N3 + ((7 * h) >> 1) * 8 + (lane & 3) * 8) * 2;
        bf16x8 xf[28];
#define RDX(p) do { _Pragma("unroll") for (int s = 0; s < 4; ++s) xf[4 * (p) + s] = *(LAS const bf16x8*)(lds + ((p) % 6) * XSLOT + (xa0 ^ (32 * s))); } while (0)
#define LDQ(p) dma16(rw, vq, ((p) * 64 + 16 * w4) * N3 * 2, lds0 + LDS_WQ + (4 * (p) + w4) * 1024)
        WAITV(4 + NST); BAR(); LDW(3); LDW(4); RDX(0);
        WAITV(6 + NST); BAR(); LDW(5); LDW(6); RDX(1);
        WAITV(8 + NST); BAR(); RDX(2);
        WAITV(6); BAR(); LDQ(0); LDQ(1); LDQ(2); RDX(3);
        WAITV(7); BAR(); LDQ(3); LDQ(4); RDX(4);
        WAITV(7); BAR(); LDQ(5); LDQ(6); RDX(5);
        WAITV(7); BAR(); RDX(6);
        STAMP(4);
        WAITV(0); sync4(misc + 3, lane);
        STAMP(5);
        {
            LAS const char* qb = lds + LDS_WQ + (h8 + q4) * 64 + 32 * blk + 8 * p4;
            f32x16 aq = {};
            bf16x8 wq[28];
#pragma unroll
            for (int s = 0; s < 6; ++s) wq[s] = tr_frag(qb + 1024 * s, qb + 1024 * s + 256);
            SB();
#pragma unroll
            for (int s = 0; s < 28; ++s) { aq = MFMA32(wq[s], xf[s], aq); SB(); if (s + 6 < 28) wq[s + 6] = tr_frag(qb + 1024 * (s + 6), qb + 1024 * (s + 6) + 256); SB(); }
#pragma unroll
            for (int i = 0; i < 16; ++i) aq[i] *= QSCALE;
#pragma unroll
            for (int i = 0; i < 4; ++i) { aq[12 + i] = (hh && !(h & 1)) ? 0.f : aq[12 + i]; aq[i] = (!hh && (h & 1)) ? 0.f : aq[i]; }
            *(LAS u32x4*)(lds + LDS_Q + w4 * 2048 + lane * 16) = piece(pack_lo(aq));
            *(LAS u32x4*)(lds + LDS_Q + w4 * 2048 + 1024 + lane * 16) = piece(pack_hi(aq));
            float qn = 0.f;
#pragma unroll
            for (int i = 0; i < 16; ++i) qn = fmaf(aq[i], aq[i], qn);
            qn += swap32(qn, hh);
            lds_max(lds0 + LDS_MISC + 8, pos_bits(qn));
        }
        STAMP(6);
        sync4(misc + 7, lane);
        if (h >= 14) __builtin_amdgcn_endpgm();
        const int c3 = h >= 7 ? h - 7 : h, kh = w4 >> 1, T3 = 4 * r + (h >= 7 ? 2 : 0) + (w4 & 1);
        wait4(misc + 8);
        STAMP(8);
        {
            const u32x4 rwo = mk_rsrc(wo, DM * DM * 2);
            const unsigned vo = (lane >> 3) * (DM * 2) + c3 * 128 + (((lane & 7) ^ (((lane >> 4) & 1) << 2)) * 16);
#pragma unroll
            for (int i = 0; i < 14; ++i) dma16(rwo, vo, (w4 + 4 * i) * 8 * DM * 2, lds0 + LDS_WO + (w4 + 4 * i) * 1024);
        }
        const u64 tag = (u64)misc[5] | ((u64)misc[6] << 32);
        const int sw = (q4 >> 1) & 1;
        unsigned b1 = LDS_WO + (kh * 8 * DH + (hh ? 4 : 0) + q4) * 128 + 64 * (sw ^ kh) + 32 * blk + 8 * p4;
        unsigned b3 = b1 + (hh ? 20 : 24) * 128;
        unsigned oin = WS_O + (T3 * 16 + 8 * kh) * 2048 + lane * 16;
        unsigned fpo = WS_OFLAG + (T3 * 16 + 8 * kh + (lane & 7)) * 8;
        LAS char* exs = lds + LDS_XY + ((w4 & 1) * 2 + (kh ^ 1)) * 4096 + lane * 16;
        LAS const char* exr = lds + LDS_XY + ((w4 & 1) * 2 + kh) * 4096 + lane * 16;
        GAS __bf16* orow = (GAS __bf16*)out + (size_t)(32 * T3 + c) * DM + c3 * 64 + kh * 32 + h8;
        PIN(orow); PIN(fpo); PIN(oin); PIN(b1); PIN(b3); PIN(exs); PIN(exr);
        if (bid == 0 && w4 == 0) {
            GAS u64* sp = (GAS u64*)(ws + WS_FLAG + (((lane >> 3) & 3) * 256 + 14) * 32 + (lane & 7) * 8);
            for (unsigned spins = 0;; ++spins) { if (__all((__hip_atomic_load(sp, RLX_AGENT) >> 16) == tag) || spins > SPIN_CAP) break; __builtin_amdgcn_s_sleep(8); }
        }
        f32x16 pa[2] = {};
        bf16x8 wf[8][2][2];
        STAMP(9);
        WAITV(0); BAR();
        STAMP(10);
        {
#pragma unroll
            for (int i = 0; i < 8; ++i)
#pragma unroll
                for (int a = 0; a < 2; ++a) {
                    LAS const char *p1 = lds + (b1 ^ (64 * a)), *p3 = lds + (b3 ^ (64 * a));
                    wf[i][0][a] = tr_frag(p1 + i * 3584, p1 + i * 3584 + 1024); wf[i][1][a] = tr_frag(p1 + i * 3584 + 2048, p3 + i * 3584);
                    PIN(wf[i][0][a]); PIN(wf[i][1][a]); }
            u64 fv, fa, fb;
            WAIT_FLAGS(fv, fa, fb, fpo, tag, "0xff", "0");
            STAMP(11);
            for (unsigned spins = 0;; ++spins) {
                MEMFENCE();
                u32x4 of[8][2];
#pragma unroll
                for (int i = 0; i < 8; ++i) { of[i][0] = __builtin_amdgcn_raw_buffer_load_b128(rws, oin, i * 2048, SC1); SB(); of[i][1] = __builtin_amdgcn_raw_buffer_load_b128(rws, oin + 1024, i * 2048, SC1); SB(); }
                asm volatile("s_waitcnt vmcnt(16)" : "+v"(fa), "+v"(fb) :: "memory");
                unsigned mx = 0;
                pa[0] = (f32x16){}; pa[1] = (f32x16){};
#pragma unroll
                for (int i = 0; i < 8; ++i)
#pragma unroll
                    for (int k = 0; k < 2; ++k) {
                        pa[1] = MFMA32(wf[i][k][1], BF(of[i][k]), pa[1]); SB();
                        mx = max(max(mx, of[i][k][0]), of[i][k][1]); mx = max(max(mx, of[i][k][2]), of[i][k][3]); PIN(mx); SB();
                        pa[0] = MFMA32(wf[i][k][0], BF(of[i][k]), pa[0]); SB();
                    }
                if (!__any(mx == SENT) || spins > SPIN_CAP) break;
            }
        }
        STAMP(12);
        {
#pragma unroll
            for (int g = 0; g < 4; ++g) *(LAS f32x4*)(exs + g * 1024) = (f32x4){pa[1][4 * g], pa[1][4 * g + 1], pa[1][4 * g + 2], pa[1][4 * g + 3]};
            BAR();
#pragma unroll
            for (int g = 0; g < 4; ++g) { const f32x4 v = *(LAS const f32x4*)(exr + g * 1024);
#pragma unroll
                for (int i = 0; i < 4; ++i) pa[0][4 * g + i] += v[i]; }
            *(GAS u32x4*)(orow) = piece(pack_lo(pa[0]));
            *(GAS u32x4*)(orow + 16) = piece(pack_hi(pa[0]));
        }
        if (bid == 0 && threadIdx.x == 256) __hip_atomic_store((GAS u64*)(ws + WS_EPOCH), tag, RLX_AGENT);
        STAMP(13);
        __builtin_amdgcn_endpgm();
    } else {
#define LDX(p) do { const unsigned sl = lds0 + LDS_X + ((p) % 6) * XSLOT + w4 * 4096; \
            dma16(rx, vx, xs0 + (p) * 128, sl); dma16(rx, vx1, xs0 + (p) * 128 + 8 * 896, sl + 1024); dma16(rx, vx, xs0 + (p) * 128 + 16 * 896, sl + 2048); dma16(rx, vx1, xs0 + (p) * 128 + 24 * 896, sl + 3072); } while (0)
        LDX(1); LDX(2);
        SB();
        const u64 epoch = __hip_atomic_load((GAS u64*)(ws + WS_EPOCH), RLX_AGENT);
        if (threadIdx.x < 12) misc[threadIdx.x] = 0u;
        unsigned ooff = WS_O + (T * 16 + h) * 2048 + lane * 16;
        PIN(ooff);
#define AUX() do { \
        __builtin_amdgcn_raw_buffer_store_b128((u32x4){SENT, SENT, SENT, SENT}, rws, ooff, 0, SC1); \
        __builtin_amdgcn_raw_buffer_store_b128((u32x4){SENT, SENT, SENT, SENT}, rws, ooff + 1024, 0, SC1); } while (0)
        const bool isk = h < 7, isv = h >= 7 && h < 14;
        const int ct = isk ? h : h - 7;
        unsigned dst0, dst1, dstp;
        { const int c0 = 64 * ct + c, h0 = (c0 * 293) >> 13, c1 = c0 + 32, h1 = (c1 * 293) >> 13;
          dst0 = WS_KV + T * TREC + (isk ? ct * 4096 + lane * 16 : TR_V + h0 * 2048 + (c0 - DH * h0 + (hh ? 32 : 0)) * 16);
          dst1 = WS_KV + T * TREC + TR_V + h1 * 2048 + (c1 - DH * h1 + (hh ? 32 : 0)) * 16;
          const int hp = (64 * ct) / DH + (lane >> 4);
          dstp = (DH * hp + DH - 1 < 64 * ct + 64) ? WS_KV + T * TREC + TR_V + hp * 2048 + ((lane >> 2) & 3) * 512 + (DH + (lane & 3)) * 16 : OOB;
          PIN(dst0); PIN(dst1); PIN(dstp); }
        const int sw = (q4 >> 1) & 1;
        const unsigned wa0 = LDS_W + (h8 + q4) * 128 + 64 * sw + 32 * blk + 8 * p4, wa1 = wa0 ^ 64;
        f32x16 a0 = {}, a1 = {};
        bf16x8 xf_[2][4], w0_[2][4], w1_[2][4];
        STAMPV(0, t_entry); STAMP(1);
#define XW(p) ((p) == 0 ? 9 + NST : (p) == 1 ? 13 + NST : (p) == 2 ? 19 + NST : 4 * (6 - (p)) + 2)
#define P1RD1(p, s) do { xf_[(p) & 1][s] = *(LAS const bf16x8*)(lds + ((p) % 6) * XSLOT + (xa0 ^ (32 * (s)))); \
            w0_[(p) & 1][s] = tr_frag(lds + wa0 + 8192 * (p) + 2048 * (s), lds + wa0 + 8192 * (p) + 2048 * (s) + 512); } while (0)
#define P1RD2(p, s) do { w1_[(p) & 1][s] = tr_frag(lds + wa1 + 8192 * (p) + 2048 * (s), lds + wa1 + 8192 * (p) + 2048 * (s) + 512); } while (0)
#define P1M(acc, wv, p, s, KT) do { if (KT) acc = MFMA32(wv[(p) & 1][s], xf_[(p) & 1][s], acc); else acc = MFMA32(xf_[(p) & 1][s], wv[(p) & 1][s], acc); } while (0)
#define P1X(p, s) (xf_[(p) & 1][s] = *(LAS const bf16x8*)(lds + ((p) % 6) * XSLOT + (xa0 ^ (32 * (s)))))
#define P1W(wv, wa, p, s) (wv[(p) & 1][s] = tr_frag(lds + wa + 8192 * (p) + 2048 * (s), lds + wa + 8192 * (p) + 2048 * (s) + 512))
#define P1MM(acc, wv, p, s, KT) do { if ((p) > 0) { P1M(acc, wv, (p) - 1, s, KT); PIN(acc); } SB(); } while (0)
#define P1STEP(p, KT) do { WAITV(XW(p)); BAR(); SB(); if (KT < 2) { \
            P1MM(a0, w0_, p, 0, KT); P1X(p, 0); P1W(w0_, wa0, p, 0); SB(); P1MM(a1, w1_, p, 0, KT); P1W(w1_, wa1, p, 0); P1X(p, 1); SB(); \
            P1MM(a0, w0_, p, 1, KT); P1W(w0_, wa0, p, 1); P1W(w1_, wa1, p, 1); SB(); P1MM(a1, w1_, p, 1, KT); P1X(p, 2); P1W(w0_, wa0, p, 2); SB(); \
            P1MM(a0, w0_, p, 2, KT); P1W(w1_, wa1, p, 2); P1X(p, 3); SB(); P1MM(a1, w1_, p, 2, KT); P1W(w0_, wa0, p, 3); P1W(w1_, wa1, p, 3); SB(); \
            P1MM(a0, w0_, p, 3, KT); } if ((p) <= 1) { LDX(2 * (p) + 3); LDX(2 * (p) + 4); } if ((p) == 1) AUX(); SB(); if (KT < 2) P1MM(a1, w1_, p, 3, KT); } while (0)
#define P1L(acc, wv, s, KT) do { P1M(acc, wv, 6, s, KT); PIN(acc); SB(); } while (0)
#define P1LAST(KT, F, d0, d1) do { P1L(a0, w0_, 0, KT); P1L(a0, w0_, 1, KT); P1L(a0, w0_, 2, KT); P1L(a0, w0_, 3, KT); P1L(a1, w1_, 0, KT); P1L(a1, w1_, 1, KT); \
            k0 = F(pack_lo(a0)); __builtin_amdgcn_raw_buffer_store_b128(k0, rws, d0, 0, SC1); SB(); P1L(a1, w1_, 2, KT); \
            k1 = F(pack_hi(a0)); __builtin_amdgcn_raw_buffer_store_b128(k1, rws, (d0) + 1024, 0, SC1); SB(); P1L(a1, w1_, 3, KT); \
            stat = sumsq(k0) + sumsq(k1); PIN(stat); SB(); \
            k2 = F(pack_lo(a1)); __builtin_amdgcn_raw_buffer_store_b128(k2, rws, d1, 0, SC1); k3 = F(pack_hi(a1)); __builtin_amdgcn_raw_buffer_store_b128(k3, rws, (d1) + 1024, 0, SC1); SB(); \
            stat += sumsq(k2) + sumsq(k3); } while (0)
#define P1ALL(KT) do { P1STEP(0, KT); P1STEP(1, KT); P1STEP(2, KT); P1STEP(3, KT); P1STEP(4, KT); P1STEP(5, KT); P1STEP(6, KT); } while (0)
        float stat = 0.f;
        u32x4 k0, k1, k2, k3;
        if (isk) {
            P1ALL(1); STAMP(3); P1LAST(1, piece, dst0, dst0 + 2048);
            STAMP(4);
            stat += swap32(stat, hh);
        } else if (isv) {
            const unsigned one = (lane & 3) == 0 ? 0x3F803F80u : 0u;
            P1ALL(0); STAMP(3);
            __builtin_amdgcn_raw_buffer_store_b128((u32x4){one, one, one, one}, rws, dstp, 0, SC1);
            P1LAST(0, , dst0, dst1);
            STAMP(4);
        } else {
            P1ALL(2); STAMP(3); STAMP(4);
        }
        lds_max(lds0 + LDS_MISC + 4, pos_bits(stat));
        GAS u64* fo = (GAS u64*)(ws + WS_FLAG + (((r >> 2) * 16 + (lane & 15)) * 16 + h) * 32 + (r & 3) * 8);
        PIN(fo);
        STAMP(5);
        asm volatile("s_waitcnt vmcnt(0) lgkmcnt(0)" ::: "memory");
        const u64 tag = (((u64)__builtin_amdgcn_readfirstlane((unsigned)(epoch >> 32)) << 32 | (unsigned)__builtin_amdgcn_readfirstlane((unsigned)epoch)) + 1ull) & 0xFFFFFFFFFFFFull;
        {
            if (threadIdx.x == 0) { misc[5] = (unsigned)tag; misc[6] = (unsigned)(tag >> 32); }
            unsigned arr = 0;
            if (lane == 0) arr = __hip_atomic_fetch_add(misc, 1u, RLX_WG);
            MEMFENCE();
            const unsigned sb = (__hip_atomic_load(misc + 1, RLX_WG) + 0xFFFFu) >> 16;
            if (__builtin_amdgcn_readfirstlane(arr) == 3u && lane < 16) (void)__hip_atomic_exchange(fo, tag << 16 | sb, RLX_AGENT);
        }
        STAMP(6);
        const int pc0 = (7 * h) >> 1, ka = pc0 >> 3, kb = (pc0 + 3) >> 3, va = (DH * h) >> 6, vb = (DH * h + DH - 1) >> 6;
        const int l4 = (lane >> 2) & 3;
        const unsigned fpo = WS_FLAG + ((w4 * 16 + r) * 16 + (l4 == 0 ? ka : l4 == 1 ? kb : l4 == 2 ? 7 + va : 7 + vb)) * 32 + (lane & 3) * 8;
        u64 fv, fa, fb;
        SB();
        f32x16 o[4] = {};
        PIN(o[0]); PIN(o[1]); PIN(o[2]); PIN(o[3]);
        wait4(misc); wait4(misc + 7);
        bf16x8 qf[4][2];
#pragma unroll
        for (int t = 0; t < 4; ++t) { const int tg = (w4 + 1 + t) & 3; qf[t][0] = *(LAS const bf16x8*)(lds + LDS_Q + tg * 2048 + lane * 16); qf[t][1] = *(LAS const bf16x8*)(lds + LDS_Q + tg * 2048 + 1024 + lane * 16); }
        const float qn = __uint_as_float(misc[2]);
        STAMP(8);
        LAS char* cmb[3];
#pragma unroll
        for (int t = 0; t < 3; ++t) cmb[t] = lds + cmb_slot(((w4 + 1 + t) & 3) * 3 + 2 - t) + lane * 16;
#define SENDQ(t, g) (*(LAS f32x4*)(cmb[t] + (g) * 1024) = (f32x4){o[t][4 * (g)], o[t][4 * (g) + 1], o[t][4 * (g) + 2], o[t][4 * (g) + 3]})
#define SEND(t, mv) do { o[t][13] = hh ? (mv) : o[t][13]; SENDQ(t, 0); SENDQ(t, 1); SENDQ(t, 2); SENDQ(t, 3); } while (0)
        WAIT_FLAGS(fv, fa, fb, fpo, tag, "0xffff", "16");
        MEMFENCE();
        STAMP(7);
        const __amdgpu_buffer_rsrc_t rk = __builtin_amdgcn_make_buffer_rsrc(ws + WS_KV + 16 * w4 * TREC + pc0 * 512, 16, 16 * TREC, 1 << 23);
        const __amdgpu_buffer_rsrc_t rv = __builtin_amdgcn_make_buffer_rsrc(ws + WS_KV + 16 * w4 * TREC + TR_V + h * 2048, 16, 16 * TREC, 1 << 23);
#define LDF(rs_, f_, so_) __builtin_amdgcn_raw_buffer_load_b128(rs_, (f_) * 1024, so_, SC1)
        u32x4 kf[4][2], vf[4][2];
#pragma unroll
        for (int b = 0; b < 4; ++b) { kf[b][0] = LDF(rk, 0, b * TREC); SB(); kf[b][1] = LDF(rk, 1, b * TREC); SB();
            if (b < 3) { vf[b][0] = LDF(rv, 0, b * TREC); SB(); vf[b][1] = LDF(rv, 1, b * TREC); SB(); } }
        vf[3][0] = (u32x4){0u, 0u, 0u, 0u}; vf[3][1] = vf[3][0];
        if (lane == 0) __hip_atomic_fetch_add(misc + 8, 1u, RLX_WG);
        SB();
        asm volatile("s_waitcnt vmcnt(14)" : "+v"(fa), "+v"(fb) :: "memory");
        unsigned sa_ = 0, sb_ = 0, sv_ = 0;
        { const unsigned s16 = (unsigned)fv << 16;
#pragma unroll
          for (int i = 0; i < 4; ++i) { sa_ = max(sa_, (unsigned)__builtin_amdgcn_readlane(s16, i)); sb_ = max(sb_, (unsigned)__builtin_amdgcn_readlane(s16, 4 + i));
              sv_ = max(sv_, max((unsigned)__builtin_amdgcn_readlane(s16, 8 + i), (unsigned)__builtin_amdgcn_readlane(s16, 12 + i))); } }
        const float kn = __uint_as_float(sa_) + (ka != kb ? __uint_as_float(sb_) : 0.f);
        int fasti = (kn * qn * 1.05f <= SAFE_T2 && __uint_as_float(sv_) <= SAFE_V2) ? 1 : 0;
        const bool fast = __builtin_amdgcn_readfirstlane(fasti) != 0;
        float m[4] = {0.f, 0.f, 0.f, 0.f};
        STAMP(9); STAMPC(14);
        if (__builtin_expect(fast, 1)) {
            f32x16 sa, sb = {};
            u32x4 plo = {}, phi = {};
            const f32x16 zero = {};
            sa = MFMA32(BF(kf[0][0]), qf[0][0], zero); sa = MFMA32(BF(kf[0][1]), qf[0][1], sa);
            SB();
#define EX(S, i) S[i] = __builtin_amdgcn_exp2f(S[i])
#define STEP(SC, SN, B, t, X1, X2, X3, X4) do { \
            constexpr int tn = ((t) + 1) & 3, bn = (t) == 3 ? ((B) + 1) & 3 : (B), tp = ((t) + 3) & 3, bp = (t) == 0 ? ((B) + 3) & 3 : (B); \
            o[tp] = MFMA32(BF(vf[bp][0]), BF(plo), o[tp]); SB(); X1; SB(); \
            phi[2] = cvt2(SN[12], SN[13]); phi[3] = cvt2(SN[14], SN[15]); PIN(phi); SB(); EX(SC, 0); EX(SC, 1); EX(SC, 2); EX(SC, 3); PIN(SC); SB(); \
            SN = MFMA32(BF(kf[bn][0]), qf[tn][0], zero); SB(); X2; SB(); \
            plo[0] = cvt2(SC[0], SC[1]); plo[1] = cvt2(SC[2], SC[3]); PIN(plo); SB(); EX(SC, 4); EX(SC, 5); EX(SC, 6); EX(SC, 7); PIN(SC); SB(); \
            SN = MFMA32(BF(kf[bn][1]), qf[tn][1], SN); SB(); X3; SB(); \
            plo[2] = cvt2(SC[4], SC[5]); plo[3] = cvt2(SC[6], SC[7]); PIN(plo); SB(); EX(SC, 8); EX(SC, 9); EX(SC, 10); EX(SC, 11); PIN(SC); SB(); \
            o[tp] = MFMA32(BF(vf[bp][1]), BF(phi), o[tp]); SB(); X4; SB(); \
            phi[0] = cvt2(SC[8], SC[9]); phi[1] = cvt2(SC[10], SC[11]); PIN(phi); SB(); EX(SC, 12); EX(SC, 13); EX(SC, 14); EX(SC, 15); PIN(SC); SB(); \
            } while (0)
#define NOP (void)0
#define PIN4(a, b, c_, d) asm volatile("" : "+v"(a), "+v"(b), "+v"(c_), "+v"(d))
#define PINS(x) asm volatile("" : "+s"(x))
#define BLOCK(B) \
            STEP(sa, sb, B, 0, vf[((B) + 3) & 3][0] = LDF(rv, 0, so), PIN4(vf[B][0], vf[B][1], kf[((B) + 1) & 3][0], kf[((B) + 1) & 3][1]), NOP, vf[((B) + 3) & 3][1] = LDF(rv, 1, so)); \
            STEP(sb, sa, B, 1, do { so += TREC; PINS(so); } while (0), NOP, NOP, NOP); \
            STEP(sa, sb, B, 2, NOP, kf[B][0] = LDF(rk, 0, so), kf[B][1] = LDF(rk, 1, so), NOP); \
            STEP(sb, sa, B, 3, NOP, NOP, NOP, NOP)
            unsigned so = 3 * TREC;
            for (int it = 0; it < 3; ++it) { BLOCK(0); BLOCK(1); BLOCK(2); BLOCK(3); }
            STEP(sa, sb, 0, 0, vf[3][0] = LDF(rv, 0, so), PIN4(vf[0][0], vf[0][1], kf[1][0], kf[1][1]), NOP, vf[3][1] = LDF(rv, 1, so));
            STEP(sb, sa, 0, 1, NOP, NOP, NOP, NOP); STEP(sa, sb, 0, 2, NOP, NOP, NOP, NOP); STEP(sb, sa, 0, 3, NOP, NOP, NOP, NOP);
            STEP(sa, sb, 1, 0, NOP, PIN4(vf[1][0], vf[1][1], kf[2][0], kf[2][1]), NOP, NOP); STEP(sb, sa, 1, 1, NOP, NOP, NOP, NOP); STEP(sa, sb, 1, 2, NOP, NOP, NOP, NOP); STEP(sb, sa, 1, 3, NOP, NOP, NOP, NOP);
            STEP(sa, sb, 2, 0, NOP, PIN4(vf[2][0], vf[2][1], kf[3][0], kf[3][1]), NOP, NOP); STEP(sb, sa, 2, 1, NOP, NOP, NOP, NOP); STEP(sa, sb, 2, 2, NOP, NOP, NOP, NOP); STEP(sb, sa, 2, 3, NOP, NOP, NOP, NOP);
            STEP(sa, sb, 3, 0, NOP, PIN4(vf[3][0], vf[3][1], kf[3][0], kf[3][1]), NOP, NOP); STEP(sb, sa, 3, 1, NOP, NOP, NOP, NOP);
            STEP(sa, sb, 3, 2, NOP, SENDQ(0, 0), SENDQ(0, 1), SENDQ(0, 2));
            STEP(sb, sa, 3, 3, SENDQ(0, 3), SENDQ(1, 0), SENDQ(1, 1), SENDQ(1, 2));
            o[3] = MFMA32(BF(vf[3][0]), BF(plo), o[3]); SB(); SENDQ(1, 3); SB();
            phi[2] = cvt2(sb[12], sb[13]); phi[3] = cvt2(sb[14], sb[15]); SB();
            SENDQ(2, 0); SB();
            o[3] = MFMA32(BF(vf[3][1]), BF(phi), o[3]); SB();
            SENDQ(2, 1); SENDQ(2, 2); SENDQ(2, 3);
        }
        PIN(fasti);
        if (__builtin_expect(__builtin_amdgcn_readfirstlane(fasti) == 0, 0)) {
#pragma unroll
            for (int t = 0; t < 4; ++t) m[t] = -INFINITY;
            for (int j = 0; j < 16; ++j) {
                kf[0][0] = LDF(rk, 0, j * TREC); kf[0][1] = LDF(rk, 1, j * TREC); vf[0][0] = LDF(rv, 0, j * TREC); vf[0][1] = LDF(rv, 1, j * TREC);
#pragma unroll
                for (int t = 0; t < 4; ++t) {
                    SB();
                    f32x16 s = {};
                    s = MFMA32(BF(kf[0][0]), qf[t][0], s);
                    s = MFMA32(BF(kf[0][1]), qf[t][1], s);
                    float bm = s[0];
#pragma unroll
                    for (int i = 1; i < 16; ++i) bm = fmaxf(bm, s[i]);
                    bm = fmaxf(bm, swap32(bm, hh));
                    const float mn = fmaxf(m[t], bm), al = __builtin_amdgcn_exp2f(m[t] - mn);
                    m[t] = mn;
                    o[t] *= al; PIN(o[t]); SB();
#pragma unroll
                    for (int i = 0; i < 16; ++i) s[i] = __builtin_amdgcn_exp2f(s[i] - mn);
                    PIN(s); SB();
                    o[t] = MFMA32(BF(vf[0][0]), BF(pack_lo(s)), o[t]);
                    o[t] = MFMA32(BF(vf[0][1]), BF(pack_hi(s)), o[t]);
                    PIN(o[t]);
                }
            }
            SEND(0, m[0]); SEND(1, m[1]); SEND(2, m[2]);
            o[3][13] = hh ? m[3] : o[3][13];
        }
        STAMPC(15); STAMP(10);
        f32x16 acc = o[3];
        BAR();
        f32x16 p[3];
#pragma unroll
        for (int d = 0; d < 3; ++d)
#pragma unroll
            for (int g = 0; g < 4; ++g) { const f32x4 v = *(LAS const f32x4*)(lds + cmb_slot(w4 * 3 + d) + g * 1024 + lane * 16);
#pragma unroll
                for (int i = 0; i < 4; ++i) p[d][4 * g + i] = v[i]; }
        const bool plain = __all(!hh || (acc[13] == 0.f && p[0][13] == 0.f && p[1][13] == 0.f && p[2][13] == 0.f));
        if (__builtin_expect(plain, 1)) {
            acc = (acc + p[0]) + (p[1] + p[2]);
        } else {
            float mm[4] = {acc[13], p[0][13], p[1][13], p[2][13]};
#pragma unroll
            for (int d = 0; d < 4; ++d) { const float s = swap32(mm[d], hh); mm[d] = hh ? mm[d] : s; }
            const float M = fmaxf(fmaxf(mm[0], mm[1]), fmaxf(mm[2], mm[3]));
            acc *= __builtin_amdgcn_exp2f(mm[0] - M);
#pragma unroll
            for (int d = 0; d < 3; ++d) acc += p[d] * __builtin_amdgcn_exp2f(mm[d + 1] - M);
        }
        const float lsw = swap32(acc[12], hh);
        const float inv = __builtin_amdgcn_rcpf(hh ? acc[12] : lsw);
        acc *= inv;
        u32x4 lo = pack_lo(acc), hi = pack_hi(acc);
        if (hh) { hi[2] = 0u; hi[3] = 0u; }
#pragma unroll
        for (int i = 0; i < 4; ++i) { lo[i] = min(lo[i], SENT - 1u); hi[i] = min(hi[i], SENT - 1u); }
        __builtin_amdgcn_raw_buffer_store_b128(lo, rws, ooff, 0, SC1);
        __builtin_amdgcn_raw_buffer_store_b128(hi, rws, ooff + 1024, 0, SC1);
        MEMFENCE();
        if (lane == 0) (void)__hip_atomic_exchange((GAS u64*)(ws + WS_OFLAG + (T * 16 + h) * 8), tag, RLX_AGENT);
        STAMP(11);
        __builtin_amdgcn_endpgm();
    }
}

extern "C" void kernel_launch(void* const* d_in, const int* in_sizes, int n_in, void* d_out, int out_size, void* d_ws, size_t ws_size, hipStream_t stream) {
    (void)hipFuncSetAttribute((const void*)k_mha, hipFuncAttributeMaxDynamicSharedMemorySize, LDS_BYTES);
    k_mha<<<256, 512, LDS_BYTES, stream>>>((const __bf16*)d_in[0], (const __bf16*)d_in[1], (const __bf16*)d_in[2], (char*)d_ws, (__bf16*)d_out);
}
```

```cpp
#include <hip/hip_runtime.h>
#include <hip/hip_bf16.h>
#include <stdint.h>

typedef __bf16 bf16x8 __attribute__((ext_vector_type(8)));
typedef __bf16 bf16x4 __attribute__((ext_vector_type(4)));
typedef __bf16 bf16x2 __attribute__((ext_vector_type(2)));
typedef float f32x16 __attribute__((ext_vector_type(16)));
typedef float f32x8 __attribute__((ext_vector_type(8)));
typedef float f32x4 __attribute__((ext_vector_type(4)));
typedef float f32x2 __attribute__((ext_vector_type(2)));
typedef unsigned u32x4 __attribute__((ext_vector_type(4)));
typedef unsigned u32x2 __attribute__((ext_vector_type(2)));
typedef short s16x4 __attribute__((ext_vector_type(4)));
typedef unsigned long long u64;
#define LAS __attribute__((address_space(3)))
#define GAS __attribute__((address_space(1)))
#define DI __device__ __forceinline__
#define MFMA32(a, b, c) __builtin_amdgcn_mfma_f32_32x32x16_bf16((a), (b), (c), 0, 0, 0)
#define SB() __builtin_amdgcn_sched_barrier(0)
#define PIN(x) asm volatile("" : "+v"(x))
#define MEMFENCE() asm volatile("" ::: "memory")
#define RLX_AGENT __ATOMIC_RELAXED, __HIP_MEMORY_SCOPE_AGENT
#define RLX_WG __ATOMIC_RELAXED, __HIP_MEMORY_SCOPE_WORKGROUP
#define SC1 16
#define BF(x) __builtin_bit_cast(bf16x8, x)
#define U4(x) __builtin_bit_cast(u32x4, x)

constexpr int SEQ = 2048, DM = 448, NH = 16, DH = 28, N3 = 1344;
constexpr float QSCALE = 0.18898223650461362f * 1.4426950408889634f;
constexpr unsigned TREC = 61440, TR_V = 28672;
constexpr unsigned WS_KV = 4096, WS_O = 4u << 20, WS_FLAG = 6u << 20, WS_OFLAG = WS_FLAG + 32768, WS_EPOCH = WS_OFLAG + 8192, WS_END = WS_EPOCH + 256;
constexpr int LDS_W = 0, LDS_X = 57344, XSLOT = 16384, LDS_WQ = LDS_X + XSLOT, LDS_WO = LDS_X, LDS_Q = 40960, LDS_MISC = 157696, LDS_XY = LDS_WO + 57344, LDS_BYTES = LDS_MISC + 64;
DI int cmb_slot(int k) { return 4096 * (k < 10 ? k : k + 2); }
constexpr float SAFE_T2 = 90.f * 90.f;
constexpr float SAFE_V2 = 1.0e12f;
constexpr int CODE_BYTES = 29696;
constexpr unsigned SPIN_CAP = 1u << 18;
constexpr unsigned SENT = 0xFFFFFFFFu, OOB = 0xFFFFFF00u;

DI bf16x8 tr_frag(LAS const char* p0, LAS const char* p1) {
    s16x4 a = __builtin_bit_cast(s16x4, __builtin_amdgcn_ds_read_tr16_b64_v4i16((LAS s16x4*)p0));
    s16x4 b = __builtin_bit_cast(s16x4, __builtin_amdgcn_ds_read_tr16_b64_v4i16((LAS s16x4*)p1));
    return __builtin_bit_cast(bf16x8, __builtin_shufflevector(a, b, 0, 1, 2, 3, 4, 5, 6, 7));
}
DI u32x4 pack_lo(const f32x16& c) { return U4(__builtin_convertvector(__builtin_shufflevector(c, c, 0, 1, 2, 3, 4, 5, 6, 7), bf16x8)); }
DI u32x4 pack_hi(const f32x16& c) { return U4(__builtin_convertvector(__builtin_shufflevector(c, c, 8, 9, 10, 11, 12, 13, 14, 15), bf16x8)); }
DI unsigned cvt2(float a, float b) { return __builtin_bit_cast(unsigned, __builtin_convertvector((f32x2){a, b}, bf16x2)); }
DI float swap32(float v, bool hi) {
    auto r = __builtin_amdgcn_permlane32_swap(__float_as_uint(v), __float_as_uint(v), false, false);
    return hi ? __uint_as_float(r[0]) : __uint_as_float(r[1]);
}
DI u32x4 piece(u32x4 v) {
    auto a = __builtin_amdgcn_permlane32_swap(v[0], v[2], false, false), b = __builtin_amdgcn_permlane32_swap(v[1], v[3], false, false);
    return (u32x4){a[0], b[0], a[1], b[1]};
}
DI float sumsq(u32x4 v) {
    float s = 0.f;
#pragma unroll
    for (int i = 0; i < 4; ++i) { const unsigned u = v[i]; const bf16x2 p = __builtin_bit_cast(bf16x2, u); s = __builtin_amdgcn_fdot2_f32_bf16(p, p, s, false); }
    return s;
}
#define WAIT_FLAGS(res, ta, tb, off, tag_, MASK, SH) do { u64 t_; unsigned n_; \
    asm volatile("s_mov_b64 exec, " MASK "\n\ts_mov_b32 %4, %8\n\t" \
                 "v_mov_b64 %1, 0\n\tglobal_atomic_add_x2 %1, %5, %1, %6 sc0\n\ts_sleep 6\n" \
                 "1:\n\tv_mov_b64 %2, 0\n\tglobal_atomic_add_x2 %2, %5, %2, %6 sc0\n\ts_waitcnt vmcnt(1)\n\t" \
                 "v_lshrrev_b64 %3, " SH ", %1\n\tv_mov_b64 %0, %1\n\tv_cmp_ne_u64 vcc, %7, %3\n\ts_cbranch_vccz 2f\n\t" \
                 "v_mov_b64 %1, 0\n\tglobal_atomic_add_x2 %1, %5, %1, %6 sc0\n\ts_waitcnt vmcnt(1)\n\t" \
                 "v_lshrrev_b64 %3, " SH ", %2\n\tv_mov_b64 %0, %2\n\tv_cmp_ne_u64 vcc, %7, %3\n\ts_cbranch_vccz 2f\n\t" \
                 "s_sub_u32 %4, %4, 1\n\ts_cbranch_scc0 1b\n" \
                 "2:\n\ts_mov_b64 exec, -1" \
                 : "=&v"(res), "=&v"(ta), "=&v"(tb), "=&v"(t_), "=&s"(n_) : "v"(off), "s"(ws), "s"(tag_), "i"(SPIN_CAP) : "memory", "vcc", "scc"); } while (0)
DI unsigned pos_bits(float v) { return (v == v) ? __float_as_uint(v) : 0x7F800000u; }
DI void dma16(u32x4 rsrc, unsigned voff, unsigned soff, unsigned lds_dst) {
    unsigned keep;
    asm volatile("s_mov_b32 %0, m0\n\ts_mov_b32 m0, %4\n\ts_nop 0\n\tbuffer_load_dwordx4 %1, %2, %3 offen lds\n\ts_mov_b32 m0, %0"
                 : "=&s"(keep) : "v"(voff), "s"(rsrc), "s"(soff), "s"(lds_dst) : "memory");
}
DI u32x4 mk_rsrc(const void* p, unsigned bytes) { const u64 a = (u64)p; return (u32x4){(unsigned)a, (unsigned)(a >> 32) & 0xffffu, bytes, 0x00020000u}; }
#define WAITV(n) asm volatile("s_waitcnt vmcnt(%c0)" :: "i"(n) : "memory")
#define BAR() asm volatile("s_waitcnt lgkmcnt(0)\n\ts_barrier" ::: "memory")
DI void lds_max(unsigned addr, unsigned v) { asm volatile("ds_max_u32 %0, %1" :: "v"(addr), "v"(v) : "memory"); }
DI void wait4(LAS unsigned* cnt) {
    for (unsigned n = 0; __builtin_amdgcn_readfirstlane(__hip_atomic_load(cnt, RLX_WG)) < 4u && n < (SPIN_CAP << 6); ++n) __builtin_amdgcn_s_sleep(1);
    MEMFENCE();
}
DI void sync4(LAS unsigned* cnt, int lane) {
    asm volatile("s_waitcnt lgkmcnt(0)" ::: "memory");
    if (lane == 0) __hip_atomic_fetch_add(cnt, 1u, RLX_WG);
    wait4(cnt);
}

__global__ __launch_bounds__(512, 2) void k_mha(const __bf16* x, const __bf16* w, const __bf16* wo, char* ws, __bf16* out) {
    extern __shared__ __attribute__((aligned(16))) char lds_[];
    u64 pc0_;
    asm volatile("s_getpc_b64 %0" : "=s"(pc0_));
    LAS char* lds = (LAS char*)lds_;
    LAS unsigned* misc = (LAS unsigned*)(lds + LDS_MISC);
    const unsigned lds0 = (unsigned)(uintptr_t)lds;
    const int bid = blockIdx.x;
    const int h = (bid & 1) * 8 + ((bid >> 3) & 7), r = ((bid >> 1) & 3) * 4 + (bid >> 6);
    const int lane = threadIdx.x & 63, wid = __builtin_amdgcn_readfirstlane(threadIdx.x >> 6), w4 = wid & 3;
    const int c = lane & 31; const bool hh = lane >= 32;
    const int T = 4 * r + w4;
    const bool roleA = wid < 4;
    const unsigned vx = (lane >> 3) * 896 + (((lane & 7) ^ (lane >> 4)) * 16), vx1 = vx ^ 64;
    const unsigned vw = (lane >> 3) * (N3 * 2) + 896 + (h < 14 ? h : 0) * 128 + (((lane & 7) ^ (((lane >> 4) & 1) << 2)) * 16);
    const unsigned xs0 = 32 * T * 896;
    unsigned ve = roleA ? vx : vw, vo = roleA ? vx1 : vw;
    unsigned s0 = roleA ? xs0 : w4 * 8 * N3 * 2, ds = roleA ? 7168 : 32 * N3 * 2, l0 = lds0 + (roleA ? LDS_X + w4 * 4096 : LDS_W + w4 * 1024), dl = roleA ? 1024 : 4096;
    asm volatile("" : "+v"(ve), "+v"(vo), "+s"(s0), "+s"(ds), "+s"(l0), "+s"(dl));
    SB();
    asm volatile("" : "+s"(x), "+s"(w), "+s"(wo), "+s"(ws), "+s"(out));
    const u32x4 rx = mk_rsrc(x, SEQ * DM * 2), rwt = mk_rsrc(w, h < 14 ? DM * N3 * 2 : 0);
    {
        const u32x4 r0 = roleA ? rx : rwt;
#pragma unroll
        for (int k = 0; k < 4; ++k) dma16(r0, (k & 1) ? vo : ve, s0 + k * ds, l0 + k * dl);
    }
    SB();
    const __amdgpu_buffer_rsrc_t rws = __builtin_amdgcn_make_buffer_rsrc(ws, 0, WS_END, 0x00020000);
    const int blk = (lane >> 4) & 1, q4 = (lane & 15) >> 2, p4 = lane & 3, h8 = hh ? 8 : 0;
    const unsigned xa0 = LDS_X + w4 * 4096 + c * 128 + (((hh ? 1 : 0) ^ ((c >> 1) & 7)) * 16);

    if (!roleA) {
        asm volatile("; role B" ::: "memory");
        const u32x4 rw = mk_rsrc(w, DM * N3 * 2);
#define LDW(p) do { dma16(rwt, vw, (8 * (p) + w4) * 8 * N3 * 2, lds0 + LDS_W + (8 * (p) + w4) * 1024); dma16(rwt, vw, (8 * (p) + 4 + w4) * 8 * N3 * 2, lds0 + LDS_W + (8 * (p) + 4 + w4) * 1024); } while (0)
        LDW(2);
        unsigned pf_;
        { const unsigned po = min((unsigned)(bid >> 3) * 1024 + w4 * 256 + (lane & 3) * 64, (unsigned)CODE_BYTES);
          asm volatile("global_load_dword %0, %1, %2" : "=v"(pf_) : "v"(po), "s"(pc0_) : "memory"); }
        const unsigned vq = ((lane >> 2) * N3 + ((7 * h) >> 1) * 8 + (lane & 3) * 8) * 2;
        bf16x8 xf[28];
#define RDX(p) do { _Pragma("unroll") for (int s = 0; s < 4; ++s) xf[4 * (p) + s] = *(LAS const bf16x8*)(lds + ((p) % 6) * XSLOT + (xa0 ^ (32 * s))); } while (0)
#define LDQ(p) dma16(rw, vq, ((p) * 64 + 16 * w4) * N3 * 2, lds0 + LDS_WQ + (4 * (p) + w4) * 1024)
        WAITV(5); BAR(); LDW(3); LDW(4); RDX(0);
        WAITV(7); BAR(); LDW(5); LDW(6); RDX(1);
        WAITV(9); BAR(); RDX(2);
        asm volatile("s_waitcnt vmcnt(6)" : "+v"(pf_) :: "memory"); BAR(); LDQ(0); LDQ(1); LDQ(2); RDX(3);
        WAITV(7); BAR(); LDQ(3); LDQ(4); RDX(4);
        WAITV(7); BAR(); LDQ(5); LDQ(6); RDX(5);
        WAITV(7); BAR(); RDX(6);
        WAITV(0); sync4(misc + 3, lane);
        {
            LAS const char* qb = lds + LDS_WQ + (h8 + q4) * 64 + 32 * blk + 8 * p4;
            f32x16 aq = {};
            bf16x8 wq[28];
#pragma unroll
            for (int s = 0; s < 6; ++s) wq[s] = tr_frag(qb + 1024 * s, qb + 1024 * s + 256);
            SB();
#pragma unroll
            for (int s = 0; s < 28; ++s) { aq = MFMA32(wq[s], xf[s], aq); SB(); if (s + 6 < 28) wq[s + 6] = tr_frag(qb + 1024 * (s + 6), qb + 1024 * (s + 6) + 256); SB(); }
#pragma unroll
            for (int i = 0; i < 16; ++i) aq[i] *= QSCALE;
#pragma unroll
            for (int i = 0; i < 4; ++i) { aq[12 + i] = (hh && !(h & 1)) ? 0.f : aq[12 + i]; aq[i] = (!hh && (h & 1)) ? 0.f : aq[i]; }
            *(LAS u32x4*)(lds + LDS_Q + w4 * 2048 + lane * 16) = piece(pack_lo(aq));
            *(LAS u32x4*)(lds + LDS_Q + w4 * 2048 + 1024 + lane * 16) = piece(pack_hi(aq));
            float qn = 0.f;
#pragma unroll
            for (int i = 0; i < 16; ++i) qn = fmaf(aq[i], aq[i], qn);
            qn += swap32(qn, hh);
            lds_max(lds0 + LDS_MISC + 8, pos_bits(qn));
        }
        sync4(misc + 7, lane);
        if (h >= 14) __builtin_amdgcn_endpgm();
        const int c3 = h >= 7 ? h - 7 : h, kh = w4 >> 1, T3 = 4 * r + (h >= 7 ? 2 : 0) + (w4 & 1);
        wait4(misc + 8);
        {
            const u32x4 rwo = mk_rsrc(wo, DM * DM * 2);
            const unsigned vo = (lane >> 3) * (DM * 2) + c3 * 128 + (((lane & 7) ^ (((lane >> 4) & 1) << 2)) * 16);
#pragma unroll
            for (int i = 0; i < 14; ++i) dma16(rwo, vo, (w4 + 4 * i) * 8 * DM * 2, lds0 + LDS_WO + (w4 + 4 * i) * 1024);
        }
        const u64 tag = (u64)misc[5] | ((u64)misc[6] << 32);
        const int sw = (q4 >> 1) & 1;
        unsigned b1 = LDS_WO + (kh * 8 * DH + (hh ? 4 : 0) + q4) * 128 + 64 * (sw ^ kh) + 32 * blk + 8 * p4;
        unsigned b3 = b1 + (hh ? 20 : 24) * 128;
        unsigned oin = WS_O + (T3 * 16 + 8 * kh) * 2048 + lane * 16;
        unsigned fpo = WS_OFLAG + (T3 * 16 + 8 * kh + (lane & 7)) * 8;
        LAS char* exs = lds + LDS_XY + ((w4 & 1) * 2 + (kh ^ 1)) * 4096 + lane * 16;
        LAS const char* exr = lds + LDS_XY + ((w4 & 1) * 2 + kh) * 4096 + lane * 16;
        GAS __bf16* orow = (GAS __bf16*)out + (size_t)(32 * T3 + c) * DM + c3 * 64 + kh * 32 + h8;
        PIN(orow); PIN(fpo); PIN(oin); PIN(b1); PIN(b3); PIN(exs); PIN(exr);
        if (bid == 0 && w4 == 0) {
            GAS u64* sp = (GAS u64*)(ws + WS_FLAG + (((lane >> 3) & 3) * 256 + 14) * 32 + (lane & 7) * 8);
            for (unsigned spins = 0;; ++spins) { if (__all((__hip_atomic_load(sp, RLX_AGENT) >> 16) == tag) || spins > SPIN_CAP) break; __builtin_amdgcn_s_sleep(8); }
        }
        f32x16 pa[2] = {};
        bf16x8 wf[8][2][2];
        WAITV(0); BAR();
        {
#pragma unroll
            for (int i = 0; i < 8; ++i)
#pragma unroll
                for (int a = 0; a < 2; ++a) {
                    LAS const char *p1 = lds + (b1 ^ (64 * a)), *p3 = lds + (b3 ^ (64 * a));
                    wf[i][0][a] = tr_frag(p1 + i * 3584, p1 + i * 3584 + 1024); wf[i][1][a] = tr_frag(p1 + i * 3584 + 2048, p3 + i * 3584);
                    PIN(wf[i][0][a]); PIN(wf[i][1][a]); }
            u64 fv, fa, fb;
            WAIT_FLAGS(fv, fa, fb, fpo, tag, "0xff", "0");
            for (unsigned spins = 0;; ++spins) {
                MEMFENCE();
                u32x4 of[8][2];
#pragma unroll
                for (int i = 0; i < 8; ++i) { of[i][0] = __builtin_amdgcn_raw_buffer_load_b128(rws, oin, i * 2048, SC1); SB(); of[i][1] = __builtin_amdgcn_raw_buffer_load_b128(rws, oin + 1024, i * 2048, SC1); SB(); }
                asm volatile("s_waitcnt vmcnt(16)" : "+v"(fa), "+v"(fb) :: "memory");
                unsigned mx = 0;
                pa[0] = (f32x16){}; pa[1] = (f32x16){};
#pragma unroll
                for (int i = 0; i < 8; ++i)
#pragma unroll
                    for (int k = 0; k < 2; ++k) {
                        pa[1] = MFMA32(wf[i][k][1], BF(of[i][k]), pa[1]); SB();
                        mx = max(max(mx, of[i][k][0]), of[i][k][1]); mx = max(max(mx, of[i][k][2]), of[i][k][3]); PIN(mx); SB();
                        pa[0] = MFMA32(wf[i][k][0], BF(of[i][k]), pa[0]); SB();
                    }
                if (!__any(mx == SENT) || spins > SPIN_CAP) break;
            }
        }
        {
#pragma unroll
            for (int g = 0; g < 4; ++g) *(LAS f32x4*)(exs + g * 1024) = (f32x4){pa[1][4 * g], pa[1][4 * g + 1], pa[1][4 * g + 2], pa[1][4 * g + 3]};
            BAR();
#pragma unroll
            for (int g = 0; g < 4; ++g) { const f32x4 v = *(LAS const f32x4*)(exr + g * 1024);
#pragma unroll
                for (int i = 0; i < 4; ++i) pa[0][4 * g + i] += v[i]; }
            *(GAS u32x4*)(orow) = piece(pack_lo(pa[0]));
            *(GAS u32x4*)(orow + 16) = piece(pack_hi(pa[0]));
        }
        if (bid == 0 && threadIdx.x == 256) __hip_atomic_store((GAS u64*)(ws + WS_EPOCH), tag, RLX_AGENT);
        __builtin_amdgcn_endpgm();
    } else {
#define LDX(p) do { const unsigned sl = lds0 + LDS_X + ((p) % 6) * XSLOT + w4 * 4096; \
            dma16(rx, vx, xs0 + (p) * 128, sl); dma16(rx, vx1, xs0 + (p) * 128 + 8 * 896, sl + 1024); dma16(rx, vx, xs0 + (p) * 128 + 16 * 896, sl + 2048); dma16(rx, vx1, xs0 + (p) * 128 + 24 * 896, sl + 3072); } while (0)
        LDX(1); LDX(2);
        SB();
        const u64 epoch = __hip_atomic_load((GAS u64*)(ws + WS_EPOCH), RLX_AGENT);
        if (threadIdx.x < 12) misc[threadIdx.x] = 0u;
        unsigned ooff = WS_O + (T * 16 + h) * 2048 + lane * 16;
        PIN(ooff);
#define AUX() do { \
        __builtin_amdgcn_raw_buffer_store_b128((u32x4){SENT, SENT, SENT, SENT}, rws, ooff, 0, SC1); \
        __builtin_amdgcn_raw_buffer_store_b128((u32x4){SENT, SENT, SENT, SENT}, rws, ooff + 1024, 0, SC1); } while (0)
        const bool isk = h < 7, isv = h >= 7 && h < 14;
        const int ct = isk ? h : h - 7;
        unsigned dst0, dst1, dstp;
        { const int c0 = 64 * ct + c, h0 = (c0 * 293) >> 13, c1 = c0 + 32, h1 = (c1 * 293) >> 13;
          dst0 = WS_KV + T * TREC + (isk ? ct * 4096 + lane * 16 : TR_V + h0 * 2048 + (c0 - DH * h0 + (hh ? 32 : 0)) * 16);
          dst1 = WS_KV + T * TREC + TR_V + h1 * 2048 + (c1 - DH * h1 + (hh ? 32 : 0)) * 16;
          const int hp = (64 * ct) / DH + (lane >> 4);
          dstp = (DH * hp + DH - 1 < 64 * ct + 64) ? WS_KV + T * TREC + TR_V + hp * 2048 + ((lane >> 2) & 3) * 512 + (DH + (lane & 3)) * 16 : OOB;
          PIN(dst0); PIN(dst1); PIN(dstp); }
        const int sw = (q4 >> 1) & 1;
        const unsigned wa0 = LDS_W + (h8 + q4) * 128 + 64 * sw + 32 * blk + 8 * p4, wa1 = wa0 ^ 64;
        f32x16 a0 = {}, a1 = {};
        bf16x8 xf_[2][4], w0_[2][4], w1_[2][4];
#define XW(p) ((p) == 0 ? 9 : (p) == 1 ? 13 : (p) == 2 ? 19 : 4 * (6 - (p)) + 2)
#define P1RD1(p, s) do { xf_[(p) & 1][s] = *(LAS const bf16x8*)(lds + ((p) % 6) * XSLOT + (xa0 ^ (32 * (s)))); \
            w0_[(p) & 1][s] = tr_frag(lds + wa0 + 8192 * (p) + 2048 * (s), lds + wa0 + 8192 * (p) + 2048 * (s) + 512); } while (0)
#define P1RD2(p, s) do { w1_[(p) & 1][s] = tr_frag(lds + wa1 + 8192 * (p) + 2048 * (s), lds + wa1 + 8192 * (p) + 2048 * (s) + 512); } while (0)
#define P1M(acc, wv, p, s, KT) do { if (KT) acc = MFMA32(wv[(p) & 1][s], xf_[(p) & 1][s], acc); else acc = MFMA32(xf_[(p) & 1][s], wv[(p) & 1][s], acc); } while (0)
#define P1X(p, s) (xf_[(p) & 1][s] = *(LAS const bf16x8*)(lds + ((p) % 6) * XSLOT + (xa0 ^ (32 * (s)))))
#define P1W(wv, wa, p, s) (wv[(p) & 1][s] = tr_frag(lds + wa + 8192 * (p) + 2048 * (s), lds + wa + 8192 * (p) + 2048 * (s) + 512))
#define P1MM(acc, wv, p, s, KT) do { if ((p) > 0) { P1M(acc, wv, (p) - 1, s, KT); PIN(acc); } SB(); } while (0)
#define P1STEP(p, KT) do { WAITV(XW(p)); BAR(); SB(); if (KT < 2) { \
            P1MM(a0, w0_, p, 0, KT); P1X(p, 0); P1W(w0_, wa0, p, 0); SB(); P1MM(a1, w1_, p, 0, KT); P1W(w1_, wa1, p, 0); P1X(p, 1); SB(); \
            P1MM(a0, w0_, p, 1, KT); P1W(w0_, wa0, p, 1); P1W(w1_, wa1, p, 1); SB(); P1MM(a1, w1_, p, 1, KT); P1X(p, 2); P1W(w0_, wa0, p, 2); SB(); \
            P1MM(a0, w0_, p, 2, KT); P1W(w1_, wa1, p, 2); P1X(p, 3); SB(); P1MM(a1, w1_, p, 2, KT); P1W(w0_, wa0, p, 3); P1W(w1_, wa1, p, 3); SB(); \
            P1MM(a0, w0_, p, 3, KT); } if ((p) <= 1) { LDX(2 * (p) + 3); LDX(2 * (p) + 4); } if ((p) == 1) AUX(); SB(); if (KT < 2) P1MM(a1, w1_, p, 3, KT); } while (0)
#define P1L(acc, wv, s, KT) do { P1M(acc, wv, 6, s, KT); PIN(acc); SB(); } while (0)
#define P1LAST(KT, F, d0, d1) do { P1L(a0, w0_, 0, KT); P1L(a0, w0_, 1, KT); P1L(a0, w0_, 2, KT); P1L(a0, w0_, 3, KT); P1L(a1, w1_, 0, KT); P1L(a1, w1_, 1, KT); \
            k0 = F(pack_lo(a0)); __builtin_amdgcn_raw_buffer_store_b128(k0, rws, d0, 0, SC1); SB(); P1L(a1, w1_, 2, KT); \
            k1 = F(pack_hi(a0)); __builtin_amdgcn_raw_buffer_store_b128(k1, rws, (d0) + 1024, 0, SC1); SB(); P1L(a1, w1_, 3, KT); \
            stat = sumsq(k0) + sumsq(k1); PIN(stat); SB(); \
            k2 = F(pack_lo(a1)); __builtin_amdgcn_raw_buffer_store_b128(k2, rws, d1, 0, SC1); k3 = F(pack_hi(a1)); __builtin_amdgcn_raw_buffer_store_b128(k3, rws, (d1) + 1024, 0, SC1); SB(); \
            stat += sumsq(k2) + sumsq(k3); } while (0)
#define P1ALL(KT) do { P1STEP(0, KT); P1STEP(1, KT); P1STEP(2, KT); P1STEP(3, KT); P1STEP(4, KT); P1STEP(5, KT); P1STEP(6, KT); } while (0)
        float stat = 0.f;
        u32x4 k0, k1, k2, k3;
        if (isk) {
            P1ALL(1); P1LAST(1, piece, dst0, dst0 + 2048);
            stat += swap32(stat, hh);
        } else if (isv) {
            const unsigned one = (lane & 3) == 0 ? 0x3F803F80u : 0u;
            P1ALL(0);
            __builtin_amdgcn_raw_buffer_store_b128((u32x4){one, one, one, one}, rws, dstp, 0, SC1);
            P1LAST(0, , dst0, dst1);
        } else {
            P1ALL(2);
        }
        lds_max(lds0 + LDS_MISC + 4, pos_bits(stat));
        GAS u64* fo = (GAS u64*)(ws + WS_FLAG + (((r >> 2) * 16 + (lane & 15)) * 16 + h) * 32 + (r & 3) * 8);
        PIN(fo);
        asm volatile("s_waitcnt vmcnt(0) lgkmcnt(0)" ::: "memory");
        const u64 tag = (((u64)__builtin_amdgcn_readfirstlane((unsigned)(epoch >> 32)) << 32 | (unsigned)__builtin_amdgcn_readfirstlane((unsigned)epoch)) + 1ull) & 0xFFFFFFFFFFFFull;
        {
            if (threadIdx.x == 0) { misc[5] = (unsigned)tag; misc[6] = (unsigned)(tag >> 32); }
            unsigned arr = 0;
            if (lane == 0) arr = __hip_atomic_fetch_add(misc, 1u, RLX_WG);
            MEMFENCE();
            const unsigned sb = (__hip_atomic_load(misc + 1, RLX_WG) + 0xFFFFu) >> 16;
            if (__builtin_amdgcn_readfirstlane(arr) == 3u && lane < 16) (void)__hip_atomic_exchange(fo, tag << 16 | sb, RLX_AGENT);
        }
        const int pc0 = (7 * h) >> 1, ka = pc0 >> 3, kb = (pc0 + 3) >> 3, va = (DH * h) >> 6, vb = (DH * h + DH - 1) >> 6;
        const int l4 = (lane >> 2) & 3;
        const unsigned fpo = WS_FLAG + ((w4 * 16 + r) * 16 + (l4 == 0 ? ka : l4 == 1 ? kb : l4 == 2 ? 7 + va : 7 + vb)) * 32 + (lane & 3) * 8;
        u64 fv, fa, fb;
        SB();
        f32x16 o[4] = {};
        PIN(o[0]); PIN(o[1]); PIN(o[2]); PIN(o[3]);
        wait4(misc); wait4(misc + 7);
        bf16x8 qf[4][2];
#pragma unroll
        for (int t = 0; t < 4; ++t) { const int tg = (w4 + 1 + t) & 3; qf[t][0] = *(LAS const bf16x8*)(lds + LDS_Q + tg * 2048 + lane * 16); qf[t][1] = *(LAS const bf16x8*)(lds + LDS_Q + tg * 2048 + 1024 + lane * 16); }
        const float qn = __uint_as_float(misc[2]);
        LAS char* cmb[3];
#pragma unroll
        for (int t = 0; t < 3; ++t) cmb[t] = lds + cmb_slot(((w4 + 1 + t) & 3) * 3 + 2 - t) + lane * 16;
#define SENDQ(t, g) (*(LAS f32x4*)(cmb[t] + (g) * 1024) = (f32x4){o[t][4 * (g)], o[t][4 * (g) + 1], o[t][4 * (g) + 2], o[t][4 * (g) + 3]})
#define SEND(t, mv) do { o[t][13] = hh ? (mv) : o[t][13]; SENDQ(t, 0); SENDQ(t, 1); SENDQ(t, 2); SENDQ(t, 3); } while (0)
        WAIT_FLAGS(fv, fa, fb, fpo, tag, "0xffff", "16");
        MEMFENCE();
        const __amdgpu_buffer_rsrc_t rk = __builtin_amdgcn_make_buffer_rsrc(ws + WS_KV + 16 * w4 * TREC + pc0 * 512, 16, 16 * TREC, 1 << 23);
        const __amdgpu_buffer_rsrc_t rv = __builtin_amdgcn_make_buffer_rsrc(ws + WS_KV + 16 * w4 * TREC + TR_V + h * 2048, 16, 16 * TREC, 1 << 23);
#define LDF(rs_, f_, so_) __builtin_amdgcn_raw_buffer_load_b128(rs_, (f_) * 1024, so_, SC1)
        u32x4 kf[4][2], vf[4][2];
#pragma unroll
        for (int b = 0; b < 4; ++b) { kf[b][0] = LDF(rk, 0, b * TREC); SB(); kf[b][1] = LDF(rk, 1, b * TREC); SB();
            if (b < 3) { vf[b][0] = LDF(rv, 0, b * TREC); SB(); vf[b][1] = LDF(rv, 1, b * TREC); SB(); } }
        vf[3][0] = (u32x4){0u, 0u, 0u, 0u}; vf[3][1] = vf[3][0];
        if (lane == 0) __hip_atomic_fetch_add(misc + 8, 1u, RLX_WG);
        SB();
        asm volatile("s_waitcnt vmcnt(14)" : "+v"(fa), "+v"(fb) :: "memory");
        unsigned sa_ = 0, sb_ = 0, sv_ = 0;
        { const unsigned s16 = (unsigned)fv << 16;
#pragma unroll
          for (int i = 0; i < 4; ++i) { sa_ = max(sa_, (unsigned)__builtin_amdgcn_readlane(s16, i)); sb_ = max(sb_, (unsigned)__builtin_amdgcn_readlane(s16, 4 + i));
              sv_ = max(sv_, max((unsigned)__builtin_amdgcn_readlane(s16, 8 + i), (unsigned)__builtin_amdgcn_readlane(s16, 12 + i))); } }
        const float kn = __uint_as_float(sa_) + (ka != kb ? __uint_as_float(sb_) : 0.f);
        int fasti = (kn * qn * 1.05f <= SAFE_T2 && __uint_as_float(sv_) <= SAFE_V2) ? 1 : 0;
        const bool fast = __builtin_amdgcn_readfirstlane(fasti) != 0;
        float m[4] = {0.f, 0.f, 0.f, 0.f};
        if (__builtin_expect(fast, 1)) {
            f32x16 sa, sb = {};
            u32x4 plo = {}, phi = {};
            const f32x16 zero = {};
            sa = MFMA32(BF(kf[0][0]), qf[0][0], zero); sa = MFMA32(BF(kf[0][1]), qf[0][1], sa);
            SB();
#define EX(S, i) S[i] = __builtin_amdgcn_exp2f(S[i])
#define STEP(SC, SN, B, t, X1, X2, X3, X4) do { \
            constexpr int tn = ((t) + 1) & 3, bn = (t) == 3 ? ((B) + 1) & 3 : (B), tp = ((t) + 3) & 3, bp = (t) == 0 ? ((B) + 3) & 3 : (B); \
            o[tp] = MFMA32(BF(vf[bp][0]), BF(plo), o[tp]); SB(); X1; SB(); \
            phi[2] = cvt2(SN[12], SN[13]); phi[3] = cvt2(SN[14], SN[15]); PIN(phi); SB(); EX(SC, 0); EX(SC, 1); EX(SC, 2); EX(SC, 3); PIN(SC); SB(); \
            SN = MFMA32(BF(kf[bn][0]), qf[tn][0], zero); SB(); X2; SB(); \
            plo[0] = cvt2(SC[0], SC[1]); plo[1] = cvt2(SC[2], SC[3]); PIN(plo); SB(); EX(SC, 4); EX(SC, 5); EX(SC, 6); EX(SC, 7); PIN(SC); SB(); \
            SN = MFMA32(BF(kf[bn][1]), qf[tn][1], SN); SB(); X3; SB(); \
            plo[2] = cvt2(SC[4], SC[5]); plo[3] = cvt2(SC[6], SC[7]); PIN(plo); SB(); EX(SC, 8); EX(SC, 9); EX(SC, 10); EX(SC, 11); PIN(SC); SB(); \
            o[tp] = MFMA32(BF(vf[bp][1]), BF(phi), o[tp]); SB(); X4; SB(); \
            phi[0] = cvt2(SC[8], SC[9]); phi[1] = cvt2(SC[10], SC[11]); PIN(phi); SB(); EX(SC, 12); EX(SC, 13); EX(SC, 14); EX(SC, 15); PIN(SC); SB(); \
            } while (0)
#define NOP (void)0
#define PIN4(a, b, c_, d) asm volatile("" : "+v"(a), "+v"(b), "+v"(c_), "+v"(d))
#define PINS(x) asm volatile("" : "+s"(x))
#define BLOCK(B) \
            STEP(sa, sb, B, 0, vf[((B) + 3) & 3][0] = LDF(rv, 0, so), PIN4(vf[B][0], vf[B][1], kf[((B) + 1) & 3][0], kf[((B) + 1) & 3][1]), NOP, vf[((B) + 3) & 3][1] = LDF(rv, 1, so)); \
            STEP(sb, sa, B, 1, do { so += TREC; PINS(so); } while (0), NOP, NOP, NOP); \
            STEP(sa, sb, B, 2, NOP, kf[B][0] = LDF(rk, 0, so), kf[B][1] = LDF(rk, 1, so), NOP); \
            STEP(sb, sa, B, 3, NOP, NOP, NOP, NOP)
            unsigned so = 3 * TREC;
            for (int it = 0; it < 3; ++it) { BLOCK(0); BLOCK(1); BLOCK(2); BLOCK(3); }
            STEP(sa, sb, 0, 0, vf[3][0] = LDF(rv, 0, so), PIN4(vf[0][0], vf[0][1], kf[1][0], kf[1][1]), NOP, vf[3][1] = LDF(rv, 1, so));
            STEP(sb, sa, 0, 1, NOP, NOP, NOP, NOP); STEP(sa, sb, 0, 2, NOP, NOP, NOP, NOP); STEP(sb, sa, 0, 3, NOP, NOP, NOP, NOP);
            STEP(sa, sb, 1, 0, NOP, PIN4(vf[1][0], vf[1][1], kf[2][0], kf[2][1]), NOP, NOP); STEP(sb, sa, 1, 1, NOP, NOP, NOP, NOP); STEP(sa, sb, 1, 2, NOP, NOP, NOP, NOP); STEP(sb, sa, 1, 3, NOP, NOP, NOP, NOP);
            STEP(sa, sb, 2, 0, NOP, PIN4(vf[2][0], vf[2][1], kf[3][0], kf[3][1]), NOP, NOP); STEP(sb, sa, 2, 1, NOP, NOP, NOP, NOP); STEP(sa, sb, 2, 2, NOP, NOP, NOP, NOP); STEP(sb, sa, 2, 3, NOP, NOP, NOP, NOP);
            STEP(sa, sb, 3, 0, NOP, PIN4(vf[3][0], vf[3][1], kf[3][0], kf[3][1]), NOP, NOP); STEP(sb, sa, 3, 1, NOP, NOP, NOP, NOP);
            STEP(sa, sb, 3, 2, NOP, SENDQ(0, 0), SENDQ(0, 1), SENDQ(0, 2));
            STEP(sb, sa, 3, 3, SENDQ(0, 3), SENDQ(1, 0), SENDQ(1, 1), SENDQ(1, 2));
            o[3] = MFMA32(BF(vf[3][0]), BF(plo), o[3]); SB(); SENDQ(1, 3); SB();
            phi[2] = cvt2(sb[12], sb[13]); phi[3] = cvt2(sb[14], sb[15]); SB();
            SENDQ(2, 0); SB();
            o[3] = MFMA32(BF(vf[3][1]), BF(phi), o[3]); SB();
            SENDQ(2, 1); SENDQ(2, 2); SENDQ(2, 3);
        }
        PIN(fasti);
        if (__builtin_expect(__builtin_amdgcn_readfirstlane(fasti) == 0, 0)) {
#pragma unroll
            for (int t = 0; t < 4; ++t) m[t] = -INFINITY;
            for (int j = 0; j < 16; ++j) {
                kf[0][0] = LDF(rk, 0, j * TREC); kf[0][1] = LDF(rk, 1, j * TREC); vf[0][0] = LDF(rv, 0, j * TREC); vf[0][1] = LDF(rv, 1, j * TREC);
#pragma unroll
                for (int t = 0; t < 4; ++t) {
                    SB();
                    f32x16 s = {};
                    s = MFMA32(BF(kf[0][0]), qf[t][0], s);
                    s = MFMA32(BF(kf[0][1]), qf[t][1], s);
                    float bm = s[0];
#pragma unroll
                    for (int i = 1; i < 16; ++i) bm = fmaxf(bm, s[i]);
                    bm = fmaxf(bm, swap32(bm, hh));
                    const float mn = fmaxf(m[t], bm), al = __builtin_amdgcn_exp2f(m[t] - mn);
                    m[t] = mn;
                    o[t] *= al; PIN(o[t]); SB();
#pragma unroll
                    for (int i = 0; i < 16; ++i) s[i] = __builtin_amdgcn_exp2f(s[i] - mn);
                    PIN(s); SB();
                    o[t] = MFMA32(BF(vf[0][0]), BF(pack_lo(s)), o[t]);
                    o[t] = MFMA32(BF(vf[0][1]), BF(pack_hi(s)), o[t]);
                    PIN(o[t]);
                }
            }
            SEND(0, m[0]); SEND(1, m[1]); SEND(2, m[2]);
            o[3][13] = hh ? m[3] : o[3][13];
        }
        f32x16 acc = o[3];
        BAR();
        f32x16 p[3];
#pragma unroll
        for (int d = 0; d < 3; ++d)
#pragma unroll
            for (int g = 0; g < 4; ++g) { const f32x4 v = *(LAS const f32x4*)(lds + cmb_slot(w4 * 3 + d) + g * 1024 + lane * 16);
#pragma unroll
                for (int i = 0; i < 4; ++i) p[d][4 * g + i] = v[i]; }
        const bool plain = __all(!hh || (acc[13] == 0.f && p[0][13] == 0.f && p[1][13] == 0.f && p[2][13] == 0.f));
        if (__builtin_expect(plain, 1)) {
            acc = (acc + p[0]) + (p[1] + p[2]);
        } else {
            float mm[4] = {acc[13], p[0][13], p[1][13], p[2][13]};
#pragma unroll
            for (int d = 0; d < 4; ++d) { const float s = swap32(mm[d], hh); mm[d] = hh ? mm[d] : s; }
            const float M = fmaxf(fmaxf(mm[0], mm[1]), fmaxf(mm[2], mm[3]));
            acc *= __builtin_amdgcn_exp2f(mm[0] - M);
#pragma unroll
            for (int d = 0; d < 3; ++d) acc += p[d] * __builtin_amdgcn_exp2f(mm[d + 1] - M);
        }
        const float lsw = swap32(acc[12], hh);
        const float inv = __builtin_amdgcn_rcpf(hh ? acc[12] : lsw);
        acc *= inv;
        u32x4 lo = pack_lo(acc), hi = pack_hi(acc);
        if (hh) { hi[2] = 0u; hi[3] = 0u; }
#pragma unroll
        for (int i = 0; i < 4; ++i) { lo[i] = min(lo[i], SENT - 1u); hi[i] = min(hi[i], SENT - 1u); }
        __builtin_amdgcn_raw_buffer_store_b128(lo, rws, ooff, 0, SC1);
        __builtin_amdgcn_raw_buffer_store_b128(hi, rws, ooff + 1024, 0, SC1);
        MEMFENCE();
        if (lane == 0) (void)__hip_atomic_exchange((GAS u64*)(ws + WS_OFLAG + (T * 16 + h) * 8), tag, RLX_AGENT);
        __builtin_amdgcn_endpgm();
    }
}

extern "C" void kernel_launch(void* const* d_in, const int* in_sizes, int n_in, void* d_out, int out_size, void* d_ws, size_t ws_size, hipStream_t stream) {
    (void)hipFuncSetAttribute((const void*)k_mha, hipFuncAttributeMaxDynamicSharedMemorySize, LDS_BYTES);
    k_mha<<<256, 512, LDS_BYTES, stream>>>((const __bf16*)d_in[0], (const __bf16*)d_in[1], (const __bf16*)d_in[2], (char*)d_ws, (__bf16*)d_out);
}
```

```cpp
#include <hip/hip_runtime.h>
#include <hip/hip_bf16.h>
#include <stdint.h>

typedef __bf16 bf16x8 __attribute__((ext_vector_type(8)));
typedef __bf16 bf16x4 __attribute__((ext_vector_type(4)));
typedef __bf16 bf16x2 __attribute__((ext_vector_type(2)));
typedef float f32x16 __attribute__((ext_vector_type(16)));
typedef float f32x8 __attribute__((ext_vector_type(8)));
typedef float f32x4 __attribute__((ext_vector_type(4)));
typedef float f32x2 __attribute__((ext_vector_type(2)));
typedef unsigned u32x4 __attribute__((ext_vector_type(4)));
typedef unsigned u32x2 __attribute__((ext_vector_type(2)));
typedef short s16x4 __attribute__((ext_vector_type(4)));
typedef unsigned long long u64;
#define LAS __attribute__((address_space(3)))
#define GAS __attribute__((address_space(1)))
#define DI __device__ __forceinline__
#define MFMA32(a, b, c) __builtin_amdgcn_mfma_f32_32x32x16_bf16((a), (b), (c), 0, 0, 0)
#define SB() __builtin_amdgcn_sched_barrier(0)
#define PIN(x) asm volatile("" : "+v"(x))
#define MEMFENCE() asm volatile("" ::: "memory")
#define RLX_AGENT __ATOMIC_RELAXED, __HIP_MEMORY_SCOPE_AGENT
#define RLX_WG __ATOMIC_RELAXED, __HIP_MEMORY_SCOPE_WORKGROUP
#define SC1 16
#define BF(x) __builtin_bit_cast(bf16x8, x)
#define U4(x) __builtin_bit_cast(u32x4, x)

constexpr int SEQ = 2048, DM = 448, NH = 16, DH = 28, N3 = 1344;
constexpr float QSCALE = 0.18898223650461362f * 1.4426950408889634f;
constexpr unsigned TREC = 61440, TR_V = 28672;
constexpr unsigned WS_KV = 4096, WS_O = 4u << 20, WS_FLAG = 6u << 20, WS_OFLAG = WS_FLAG + 32768, WS_EPOCH = WS_OFLAG + 8192, WS_END = WS_EPOCH + 256;
constexpr int LDS_W = 0, LDS_X = 57344, XSLOT = 16384, LDS_WQ = LDS_X + XSLOT, LDS_WO = LDS_X, LDS_Q = 40960, LDS_MISC = 157696, LDS_XY = LDS_WO + 57344, LDS_BYTES = LDS_MISC + 64 + 256;
DI int cmb_slot(int k) { return 4096 * (k < 10 ? k : k + 2); }
constexpr float SAFE_T2 = 90.f * 90.f;
constexpr float SAFE_V2 = 1.0e12f;
constexpr int CODE_BYTES = 29696;
constexpr unsigned SPIN_CAP = 1u << 18;
constexpr unsigned SENT = 0xFFFFFFFFu, OOB = 0xFFFFFF00u;

DI bf16x8 tr_frag(LAS const char* p0, LAS const char* p1) {
    s16x4 a = __builtin_bit_cast(s16x4, __builtin_amdgcn_ds_read_tr16_b64_v4i16((LAS s16x4*)p0));
    s16x4 b = __builtin_bit_cast(s16x4, __builtin_amdgcn_ds_read_tr16_b64_v4i16((LAS s16x4*)p1));
    return __builtin_bit_cast(bf16x8, __builtin_shufflevector(a, b, 0, 1, 2, 3, 4, 5, 6, 7));
}
DI u32x4 pack_lo(const f32x16& c) { return U4(__builtin_convertvector(__builtin_shufflevector(c, c, 0, 1, 2, 3, 4, 5, 6, 7), bf16x8)); }
DI u32x4 pack_hi(const f32x16& c) { return U4(__builtin_convertvector(__builtin_shufflevector(c, c, 8, 9, 10, 11, 12, 13, 14, 15), bf16x8)); }
DI unsigned cvt2(float a, float b) { return __builtin_bit_cast(unsigned, __builtin_convertvector((f32x2){a, b}, bf16x2)); }
DI float swap32(float v, bool hi) {
    auto r = __builtin_amdgcn_permlane32_swap(__float_as_uint(v), __float_as_uint(v), false, false);
    return hi ? __uint_as_float(r[0]) : __uint_as_float(r[1]);
}
DI u32x4 piece(u32x4 v) {
    auto a = __builtin_amdgcn_permlane32_swap(v[0], v[2], false, false), b = __builtin_amdgcn_permlane32_swap(v[1], v[3], false, false);
    return (u32x4){a[0], b[0], a[1], b[1]};
}
DI float sumsq(u32x4 v) {
    float s = 0.f;
#pragma unroll
    for (int i = 0; i < 4; ++i) { const unsigned u = v[i]; const bf16x2 p = __builtin_bit_cast(bf16x2, u); s = __builtin_amdgcn_fdot2_f32_bf16(p, p, s, false); }
    return s;
}
#define WAIT_FLAGS(res, ta, tb, off, tag_, MASK, SH) do { u64 t_; unsigned n_; \
    asm volatile("s_mov_b64 exec, " MASK "\n\ts_mov_b32 %4, %8\n\t" \
                 "v_mov_b64 %1, 0\n\tglobal_atomic_add_x2 %1, %5, %1, %6 sc0\n\ts_sleep 6\n" \
                 "1:\n\tv_mov_b64 %2, 0\n\tglobal_atomic_add_x2 %2, %5, %2, %6 sc0\n\ts_waitcnt vmcnt(1)\n\t" \
                 "v_lshrrev_b64 %3, " SH ", %1\n\tv_mov_b64 %0, %1\n\tv_cmp_ne_u64 vcc, %7, %3\n\ts_cbranch_vccz 2f\n\t" \
                 "v_mov_b64 %1, 0\n\tglobal_atomic_add_x2 %1, %5, %1, %6 sc0\n\ts_waitcnt vmcnt(1)\n\t" \
                 "v_lshrrev_b64 %3, " SH ", %2\n\tv_mov_b64 %0, %2\n\tv_cmp_ne_u64 vcc, %7, %3\n\ts_cbranch_vccz 2f\n\t" \
                 "s_sub_u32 %4, %4, 1\n\ts_cbranch_scc0 1b\n" \
                 "2:\n\ts_mov_b64 exec, -1" \
                 : "=&v"(res), "=&v"(ta), "=&v"(tb), "=&v"(t_), "=&s"(n_) : "v"(off), "s"(ws), "s"(tag_), "i"(SPIN_CAP) : "memory", "vcc", "scc"); } while (0)
DI unsigned pos_bits(float v) { return (v == v) ? __float_as_uint(v) : 0x7F800000u; }
DI void dma16(u32x4 rsrc, unsigned voff, unsigned soff, unsigned lds_dst) {
    unsigned keep;
    asm volatile("s_mov_b32 %0, m0\n\ts_mov_b32 m0, %4\n\ts_nop 0\n\tbuffer_load_dwordx4 %1, %2, %3 offen lds\n\ts_mov_b32 m0, %0"
                 : "=&s"(keep) : "v"(voff), "s"(rsrc), "s"(soff), "s"(lds_dst) : "memory");
}
DI u32x4 mk_rsrc(const void* p, unsigned bytes) { const u64 a = (u64)p; return (u32x4){(unsigned)a, (unsigned)(a >> 32) & 0xffffu, bytes, 0x00020000u}; }
#define WAITV(n) asm volatile("s_waitcnt vmcnt(%c0)" :: "i"(n) : "memory")
#define BAR() asm volatile("s_waitcnt lgkmcnt(0)\n\ts_barrier" ::: "memory")
DI void lds_max(unsigned addr, unsigned v) { asm volatile("ds_max_u32 %0, %1" :: "v"(addr), "v"(v) : "memory"); }
DI void wait4(LAS unsigned* cnt) {
    for (unsigned n = 0; __builtin_amdgcn_readfirstlane(__hip_atomic_load(cnt, RLX_WG)) < 4u && n < (SPIN_CAP << 6); ++n) __builtin_amdgcn_s_sleep(1);
    MEMFENCE();
}
DI void sync4(LAS unsigned* cnt, int lane) {
    asm volatile("s_waitcnt lgkmcnt(0)" ::: "memory");
    if (lane == 0) __hip_atomic_fetch_add(cnt, 1u, RLX_WG);
    wait4(cnt);
}

__global__ __launch_bounds__(512, 2) void k_mha(const __bf16* x, const __bf16* w, const __bf16* wo, char* ws, __bf16* out) {
    extern __shared__ __attribute__((aligned(16))) char lds_[];
    { u64 pc_; unsigned keep; const unsigned po = min((blockIdx.x >> 3) * 1024u + (threadIdx.x >> 5) * 64u, (unsigned)CODE_BYTES);
      asm volatile("s_mov_b32 %1, m0\n\ts_mov_b32 m0, %3\n\ts_getpc_b64 %0\n\tglobal_load_lds_dword %2, %0\n\ts_mov_b32 m0, %1"
                   : "=&s"(pc_), "=&s"(keep) : "v"(po), "s"((unsigned)(uintptr_t)(LAS char*)lds_ + LDS_MISC + 64) : "memory"); }
    LAS char* lds = (LAS char*)lds_;
    LAS unsigned* misc = (LAS unsigned*)(lds + LDS_MISC);
    const unsigned lds0 = (unsigned)(uintptr_t)lds;
    const int bid = blockIdx.x;
    const int h = (bid & 1) * 8 + ((bid >> 3) & 7), r = ((bid >> 1) & 3) * 4 + (bid >> 6);
    const int lane = threadIdx.x & 63, wid = __builtin_amdgcn_readfirstlane(threadIdx.x >> 6), w4 = wid & 3;
    const int c = lane & 31; const bool hh = lane >= 32;
    const int T = 4 * r + w4;
    const bool roleA = wid < 4;
    const unsigned vx = (lane >> 3) * 896 + (((lane & 7) ^ (lane >> 4)) * 16), vx1 = vx ^ 64;
    const unsigned vw = (lane >> 3) * (N3 * 2) + 896 + (h < 14 ? h : 0) * 128 + (((lane & 7) ^ (((lane >> 4) & 1) << 2)) * 16);
    const unsigned xs0 = 32 * T * 896;
    unsigned ve = roleA ? vx : vw, vo = roleA ? vx1 : vw;
    unsigned s0 = roleA ? xs0 : w4 * 8 * N3 * 2, ds = roleA ? 7168 : 32 * N3 * 2, l0 = lds0 + (roleA ? LDS_X + w4 * 4096 : LDS_W + w4 * 1024), dl = roleA ? 1024 : 4096;
    asm volatile("" : "+v"(ve), "+v"(vo), "+s"(s0), "+s"(ds), "+s"(l0), "+s"(dl));
    SB();
    asm volatile("" : "+s"(x), "+s"(w), "+s"(wo), "+s"(ws), "+s"(out));
    const u32x4 rx = mk_rsrc(x, SEQ * DM * 2), rwt = mk_rsrc(w, h < 14 ? DM * N3 * 2 : 0);
    {
        const u32x4 r0 = roleA ? rx : rwt;
#pragma unroll
        for (int k = 0; k < 4; ++k) dma16(r0, (k & 1) ? vo : ve, s0 + k * ds, l0 + k * dl);
    }
    SB();
    const __amdgpu_buffer_rsrc_t rws = __builtin_amdgcn_make_buffer_rsrc(ws, 0, WS_END, 0x00020000);
    const int blk = (lane >> 4) & 1, q4 = (lane & 15) >> 2, p4 = lane & 3, h8 = hh ? 8 : 0;
    const unsigned xa0 = LDS_X + w4 * 4096 + c * 128 + (((hh ? 1 : 0) ^ ((c >> 1) & 7)) * 16);

    if (!roleA) {
        asm volatile("; role B" ::: "memory");
        const u32x4 rw = mk_rsrc(w, DM * N3 * 2);
#define LDW(p) do { dma16(rwt, vw, (8 * (p) + w4) * 8 * N3 * 2, lds0 + LDS_W + (8 * (p) + w4) * 1024); dma16(rwt, vw, (8 * (p) + 4 + w4) * 8 * N3 * 2, lds0 + LDS_W + (8 * (p) + 4 + w4) * 1024); } while (0)
        LDW(2);
        const unsigned vq = ((lane >> 2) * N3 + ((7 * h) >> 1) * 8 + (lane & 3) * 8) * 2;
        bf16x8 xf[28];
#define RDX(p) do { _Pragma("unroll") for (int s = 0; s < 4; ++s) xf[4 * (p) + s] = *(LAS const bf16x8*)(lds + ((p) % 6) * XSLOT + (xa0 ^ (32 * s))); } while (0)
#define LDQ(p) dma16(rw, vq, ((p) * 64 + 16 * w4) * N3 * 2, lds0 + LDS_WQ + (4 * (p) + w4) * 1024)
        WAITV(4); BAR(); LDW(3); LDW(4); RDX(0);
        WAITV(6); BAR(); LDW(5); LDW(6); RDX(1);
        WAITV(8); BAR(); RDX(2);
        WAITV(6); BAR(); LDQ(0); LDQ(1); LDQ(2); RDX(3);
        WAITV(7); BAR(); LDQ(3); LDQ(4); RDX(4);
        WAITV(7); BAR(); LDQ(5); LDQ(6); RDX(5);
        WAITV(7); BAR(); RDX(6);
        WAITV(0); sync4(misc + 3, lane);
        {
            LAS const char* qb = lds + LDS_WQ + (h8 + q4) * 64 + 32 * blk + 8 * p4;
            f32x16 aq = {};
            bf16x8 wq[28];
#pragma unroll
            for (int s = 0; s < 6; ++s) wq[s] = tr_frag(qb + 1024 * s, qb + 1024 * s + 256);
            SB();
#pragma unroll
            for (int s = 0; s < 28; ++s) { aq = MFMA32(wq[s], xf[s], aq); SB(); if (s + 6 < 28) wq[s + 6] = tr_frag(qb + 1024 * (s + 6), qb + 1024 * (s + 6) + 256); SB(); }
#pragma unroll
            for (int i = 0; i < 16; ++i) aq[i] *= QSCALE;
#pragma unroll
            for (int i = 0; i < 4; ++i) { aq[12 + i] = (hh && !(h & 1)) ? 0.f : aq[12 + i]; aq[i] = (!hh && (h & 1)) ? 0.f : aq[i]; }
            *(LAS u32x4*)(lds + LDS_Q + w4 * 2048 + lane * 16) = piece(pack_lo(aq));
            *(LAS u32x4*)(lds + LDS_Q + w4 * 2048 + 1024 + lane * 16) = piece(pack_hi(aq));
            float qn = 0.f;
#pragma unroll
            for (int i = 0; i < 16; ++i) qn = fmaf(aq[i], aq[i], qn);
            qn += swap32(qn, hh);
            lds_max(lds0 + LDS_MISC + 8, pos_bits(qn));
        }
        sync4(misc + 7, lane);
        if (h >= 14) __builtin_amdgcn_endpgm();
        const int c3 = h >= 7 ? h - 7 : h, kh = w4 >> 1, T3 = 4 * r + (h >= 7 ? 2 : 0) + (w4 & 1);
        wait4(misc + 8);
        {
            const u32x4 rwo = mk_rsrc(wo, DM * DM * 2);
            const unsigned vo = (lane >> 3) * (DM * 2) + c3 * 128 + (((lane & 7) ^ (((lane >> 4) & 1) << 2)) * 16);
#pragma unroll
            for (int i = 0; i < 14; ++i) dma16(rwo, vo, (w4 + 4 * i) * 8 * DM * 2, lds0 + LDS_WO + (w4 + 4 * i) * 1024);
        }
        const u64 tag = (u64)misc[5] | ((u64)misc[6] << 32);
        const int sw = (q4 >> 1) & 1;
        unsigned b1 = LDS_WO + (kh * 8 * DH + (hh ? 4 : 0) + q4) * 128 + 64 * (sw ^ kh) + 32 * blk + 8 * p4;
        unsigned b3 = b1 + (hh ? 20 : 24) * 128;
        unsigned oin = WS_O + (T3 * 16 + 8 * kh) * 2048 + lane * 16;
        unsigned fpo = WS_OFLAG + (T3 * 16 + 8 * kh + (lane & 7)) * 8;
        LAS char* exs = lds + LDS_XY + ((w4 & 1) * 2 + (kh ^ 1)) * 4096 + lane * 16;
        LAS const char* exr = lds + LDS_XY + ((w4 & 1) * 2 + kh) * 4096 + lane * 16;
        GAS __bf16* orow = (GAS __bf16*)out + (size_t)(32 * T3 + c) * DM + c3 * 64 + kh * 32 + h8;
        PIN(orow); PIN(fpo); PIN(oin); PIN(b1); PIN(b3); PIN(exs); PIN(exr);
        if (bid == 0 && w4 == 0) {
            GAS u64* sp = (GAS u64*)(ws + WS_FLAG + (((lane >> 3) & 3) * 256 + 14) * 32 + (lane & 7) * 8);
            for (unsigned spins = 0;; ++spins) { if (__all((__hip_atomic_load(sp, RLX_AGENT) >> 16) == tag) || spins > SPIN_CAP) break; __builtin_amdgcn_s_sleep(8); }
        }
        f32x16 pa[2] = {};
        bf16x8 wf[8][2][2];
        WAITV(0); BAR();
        {
#pragma unroll
            for (int i = 0; i < 8; ++i)
#pragma unroll
                for (int a = 0; a < 2; ++a) {
                    LAS const char *p1 = lds + (b1 ^ (64 * a)), *p3 = lds + (b3 ^ (64 * a));
                    wf[i][0][a] = tr_frag(p1 + i * 3584, p1 + i * 3584 + 1024); wf[i][1][a] = tr_frag(p1 + i * 3584 + 2048, p3 + i * 3584);
                    PIN(wf[i][0][a]); PIN(wf[i][1][a]); }
            u64 fv, fa, fb;
            WAIT_FLAGS(fv, fa, fb, fpo, tag, "0xff", "0");
            for (unsigned spins = 0;; ++spins) {
                MEMFENCE();
                u32x4 of[8][2];
#pragma unroll
                for (int i = 0; i < 8; ++i) { of[i][0] = __builtin_amdgcn_raw_buffer_load_b128(rws, oin, i * 2048, SC1); SB(); of[i][1] = __builtin_amdgcn_raw_buffer_load_b128(rws, oin + 1024, i * 2048, SC1); SB(); }
                asm volatile("s_waitcnt vmcnt(16)" : "+v"(fa), "+v"(fb) :: "memory");
                unsigned mx = 0;
                pa[0] = (f32x16){}; pa[1] = (f32x16){};
#pragma unroll
                for (int i = 0; i < 8; ++i)
#pragma unroll
                    for (int k = 0; k < 2; ++k) {
                        pa[1] = MFMA32(wf[i][k][1], BF(of[i][k]), pa[1]); SB();
                        mx = max(max(mx, of[i][k][0]), of[i][k][1]); mx = max(max(mx, of[i][k][2]), of[i][k][3]); PIN(mx); SB();
                        pa[0] = MFMA32(wf[i][k][0], BF(of[i][k]), pa[0]); SB();
                    }
                if (!__any(mx == SENT) || spins > SPIN_CAP) break;
            }
        }
        {
#pragma unroll
            for (int g = 0; g < 4; ++g) *(LAS f32x4*)(exs + g * 1024) = (f32x4){pa[1][4 * g], pa[1][4 * g + 1], pa[1][4 * g + 2], pa[1][4 * g + 3]};
            BAR();
#pragma unroll
            for (int g = 0; g < 4; ++g) { const f32x4 v = *(LAS const f32x4*)(exr + g * 1024);
#pragma unroll
                for (int i = 0; i < 4; ++i) pa[0][4 * g + i] += v[i]; }
            *(GAS u32x4*)(orow) = piece(pack_lo(pa[0]));
            *(GAS u32x4*)(orow + 16) = piece(pack_hi(pa[0]));
        }
        if (bid == 0 && threadIdx.x == 256) __hip_atomic_store((GAS u64*)(ws + WS_EPOCH), tag, RLX_AGENT);
        __builtin_amdgcn_endpgm();
    } else {
#define LDX(p) do { const unsigned sl = lds0 + LDS_X + ((p) % 6) * XSLOT + w4 * 4096; \
            dma16(rx, vx, xs0 + (p) * 128, sl); dma16(rx, vx1, xs0 + (p) * 128 + 8 * 896, sl + 1024); dma16(rx, vx, xs0 + (p) * 128 + 16 * 896, sl + 2048); dma16(rx, vx1, xs0 + (p) * 128 + 24 * 896, sl + 3072); } while (0)
        LDX(1); LDX(2);
        SB();
        const u64 epoch = __hip_atomic_load((GAS u64*)(ws + WS_EPOCH), RLX_AGENT);
        if (threadIdx.x < 12) misc[threadIdx.x] = 0u;
        unsigned ooff = WS_O + (T * 16 + h) * 2048 + lane * 16;
        PIN(ooff);
#define AUX() do { \
        __builtin_amdgcn_raw_buffer_store_b128((u32x4){SENT, SENT, SENT, SENT}, rws, ooff, 0, SC1); \
        __builtin_amdgcn_raw_buffer_store_b128((u32x4){SENT, SENT, SENT, SENT}, rws, ooff + 1024, 0, SC1); } while (0)
        const bool isk = h < 7, isv = h >= 7 && h < 14;
        const int ct = isk ? h : h - 7;
        unsigned dst0, dst1, dstp;
        { const int c0 = 64 * ct + c, h0 = (c0 * 293) >> 13, c1 = c0 + 32, h1 = (c1 * 293) >> 13;
          dst0 = WS_KV + T * TREC + (isk ? ct * 4096 + lane * 16 : TR_V + h0 * 2048 + (c0 - DH * h0 + (hh ? 32 : 0)) * 16);
          dst1 = WS_KV + T * TREC + TR_V + h1 * 2048 + (c1 - DH * h1 + (hh ? 32 : 0)) * 16;
          const int hp = (64 * ct) / DH + (lane >> 4);
          dstp = (DH * hp + DH - 1 < 64 * ct + 64) ? WS_KV + T * TREC + TR_V + hp * 2048 + ((lane >> 2) & 3) * 512 + (DH + (lane & 3)) * 16 : OOB;
          PIN(dst0); PIN(dst1); PIN(dstp); }
        const int sw = (q4 >> 1) & 1;
        const unsigned wa0 = LDS_W + (h8 + q4) * 128 + 64 * sw + 32 * blk + 8 * p4, wa1 = wa0 ^ 64;
        f32x16 a0 = {}, a1 = {};
        bf16x8 xf_[2][4], w0_[2][4], w1_[2][4];
#define XW(p) ((p) == 0 ? 9 : (p) == 1 ? 13 : (p) == 2 ? 19 : 4 * (6 - (p)) + 2)
#define P1RD1(p, s) do { xf_[(p) & 1][s] = *(LAS const bf16x8*)(lds + ((p) % 6) * XSLOT + (xa0 ^ (32 * (s)))); \
            w0_[(p) & 1][s] = tr_frag(lds + wa0 + 8192 * (p) + 2048 * (s), lds + wa0 + 8192 * (p) + 2048 * (s) + 512); } while (0)
#define P1RD2(p, s) do { w1_[(p) & 1][s] = tr_frag(lds + wa1 + 8192 * (p) + 2048 * (s), lds + wa1 + 8192 * (p) + 2048 * (s) + 512); } while (0)
#define P1M(acc, wv, p, s, KT) do { if (KT) acc = MFMA32(wv[(p) & 1][s], xf_[(p) & 1][s], acc); else acc = MFMA32(xf_[(p) & 1][s], wv[(p) & 1][s], acc); } while (0)
#define P1X(p, s) (xf_[(p) & 1][s] = *(LAS const bf16x8*)(lds + ((p) % 6) * XSLOT + (xa0 ^ (32 * (s)))))
#define P1W(wv, wa, p, s) (wv[(p) & 1][s] = tr_frag(lds + wa + 8192 * (p) + 2048 * (s), lds + wa + 8192 * (p) + 2048 * (s) + 512))
#define P1MM(acc, wv, p, s, KT) do { if ((p) > 0) { P1M(acc, wv, (p) - 1, s, KT); PIN(acc); } SB(); } while (0)
#define P1STEP(p, KT) do { WAITV(XW(p)); BAR(); SB(); if (KT < 2) { \
            P1MM(a0, w0_, p, 0, KT); P1X(p, 0); P1W(w0_, wa0, p, 0); SB(); P1MM(a1, w1_, p, 0, KT); P1W(w1_, wa1, p, 0); P1X(p, 1); SB(); \
            P1MM(a0, w0_, p, 1, KT); P1W(w0_, wa0, p, 1); P1W(w1_, wa1, p, 1); SB(); P1MM(a1, w1_, p, 1, KT); P1X(p, 2); P1W(w0_, wa0, p, 2); SB(); \
            P1MM(a0, w0_, p, 2, KT); P1W(w1_, wa1, p, 2); P1X(p, 3); SB(); P1MM(a1, w1_, p, 2, KT); P1W(w0_, wa0, p, 3); P1W(w1_, wa1, p, 3); SB(); \
            P1MM(a0, w0_, p, 3, KT); } if ((p) <= 1) { LDX(2 * (p) + 3); LDX(2 * (p) + 4); } if ((p) == 1) AUX(); SB(); if (KT < 2) P1MM(a1, w1_, p, 3, KT); } while (0)
#define P1L(acc, wv, s, KT) do { P1M(acc, wv, 6, s, KT); PIN(acc); SB(); } while (0)
#define P1LAST(KT, F, d0, d1) do { P1L(a0, w0_, 0, KT); P1L(a0, w0_, 1, KT); P1L(a0, w0_, 2, KT); P1L(a0, w0_, 3, KT); P1L(a1, w1_, 0, KT); P1L(a1, w1_, 1, KT); \
            k0 = F(pack_lo(a0)); __builtin_amdgcn_raw_buffer_store_b128(k0, rws, d0, 0, SC1); SB(); P1L(a1, w1_, 2, KT); \
            k1 = F(pack_hi(a0)); __builtin_amdgcn_raw_buffer_store_b128(k1, rws, (d0) + 1024, 0, SC1); SB(); P1L(a1, w1_, 3, KT); \
            stat = sumsq(k0) + sumsq(k1); PIN(stat); SB(); \
            k2 = F(pack_lo(a1)); __builtin_amdgcn_raw_buffer_store_b128(k2, rws, d1, 0, SC1); k3 = F(pack_hi(a1)); __builtin_amdgcn_raw_buffer_store_b128(k3, rws, (d1) + 1024, 0, SC1); SB(); \
            stat += sumsq(k2) + sumsq(k3); } while (0)
#define P1ALL(KT) do { P1STEP(0, KT); P1STEP(1, KT); P1STEP(2, KT); P1STEP(3, KT); P1STEP(4, KT); P1STEP(5, KT); P1STEP(6, KT); } while (0)
        float stat = 0.f;
        u32x4 k0, k1, k2, k3;
        if (isk) {
            P1ALL(1); P1LAST(1, piece, dst0, dst0 + 2048);
            stat += swap32(stat, hh);
        } else if (isv) {
            const unsigned one = (lane & 3) == 0 ? 0x3F803F80u : 0u;
            P1ALL(0);
            __builtin_amdgcn_raw_buffer_store_b128((u32x4){one, one, one, one}, rws, dstp, 0, SC1);
            P1LAST(0, , dst0, dst1);
        } else {
            P1ALL(2);
        }
        lds_max(lds0 + LDS_MISC + 4, pos_bits(stat));
        GAS u64* fo = (GAS u64*)(ws + WS_FLAG + (((r >> 2) * 16 + (lane & 15)) * 16 + h) * 32 + (r & 3) * 8);
        PIN(fo);
        asm volatile("s_waitcnt vmcnt(0) lgkmcnt(0)" ::: "memory");
        const u64 tag = (((u64)__builtin_amdgcn_readfirstlane((unsigned)(epoch >> 32)) << 32 | (unsigned)__builtin_amdgcn_readfirstlane((unsigned)epoch)) + 1ull) & 0xFFFFFFFFFFFFull;
        {
            if (threadIdx.x == 0) { misc[5] = (unsigned)tag; misc[6] = (unsigned)(tag >> 32); }
            unsigned arr = 0;
            if (lane == 0) arr = __hip_atomic_fetch_add(misc, 1u, RLX_WG);
            MEMFENCE();
            const unsigned sb = (__hip_atomic_load(misc + 1, RLX_WG) + 0xFFFFu) >> 16;
            if (__builtin_amdgcn_readfirstlane(arr) == 3u && lane < 16) (void)__hip_atomic_exchange(fo, tag << 16 | sb, RLX_AGENT);
        }
        const int pc0 = (7 * h) >> 1, ka = pc0 >> 3, kb = (pc0 + 3) >> 3, va = (DH * h) >> 6, vb = (DH * h + DH - 1) >> 6;
        const int l4 = (lane >> 2) & 3;
        const unsigned fpo = WS_FLAG + ((w4 * 16 + r) * 16 + (l4 == 0 ? ka : l4 == 1 ? kb : l4 == 2 ? 7 + va : 7 + vb)) * 32 + (lane & 3) * 8;
        u64 fv, fa, fb;
        SB();
        f32x16 o[4] = {};
        PIN(o[0]); PIN(o[1]); PIN(o[2]); PIN(o[3]);
        wait4(misc); wait4(misc + 7);
        bf16x8 qf[4][2];
#pragma unroll
        for (int t = 0; t < 4; ++t) { const int tg = (w4 + 1 + t) & 3; qf[t][0] = *(LAS const bf16x8*)(lds + LDS_Q + tg * 2048 + lane * 16); qf[t][1] = *(LAS const bf16x8*)(lds + LDS_Q + tg * 2048 + 1024 + lane * 16); }
        const float qn = __uint_as_float(misc[2]);
        LAS char* cmb[3];
#pragma unroll
        for (int t = 0; t < 3; ++t) cmb[t] = lds + cmb_slot(((w4 + 1 + t) & 3) * 3 + 2 - t) + lane * 16;
#define SENDQ(t, g) (*(LAS f32x4*)(cmb[t] + (g) * 1024) = (f32x4){o[t][4 * (g)], o[t][4 * (g) + 1], o[t][4 * (g) + 2], o[t][4 * (g) + 3]})
#define SEND(t, mv) do { o[t][13] = hh ? (mv) : o[t][13]; SENDQ(t, 0); SENDQ(t, 1); SENDQ(t, 2); SENDQ(t, 3); } while (0)
        WAIT_FLAGS(fv, fa, fb, fpo, tag, "0xffff", "16");
        MEMFENCE();
        const __amdgpu_buffer_rsrc_t rk = __builtin_amdgcn_make_buffer_rsrc(ws + WS_KV + 16 * w4 * TREC + pc0 * 512, 16, 16 * TREC, 1 << 23);
        const __amdgpu_buffer_rsrc_t rv = __builtin_amdgcn_make_buffer_rsrc(ws + WS_KV + 16 * w4 * TREC + TR_V + h * 2048, 16, 16 * TREC, 1 << 23);
#define LDF(rs_, f_, so_) __builtin_amdgcn_raw_buffer_load_b128(rs_, (f_) * 1024, so_, SC1)
        u32x4 kf[4][2], vf[4][2];
#pragma unroll
        for (int b = 0; b < 4; ++b) { kf[b][0] = LDF(rk, 0, b * TREC); SB(); kf[b][1] = LDF(rk, 1, b * TREC); SB();
            if (b < 3) { vf[b][0] = LDF(rv, 0, b * TREC); SB(); vf[b][1] = LDF(rv, 1, b * TREC); SB(); } }
        vf[3][0] = (u32x4){0u, 0u, 0u, 0u}; vf[3][1] = vf[3][0];
        if (lane == 0) __hip_atomic_fetch_add(misc + 8, 1u, RLX_WG);
        SB();
        asm volatile("s_waitcnt vmcnt(14)" : "+v"(fa), "+v"(fb) :: "memory");
        unsigned sa_ = 0, sb_ = 0, sv_ = 0;
        { const unsigned s16 = (unsigned)fv << 16;
#pragma unroll
          for (int i = 0; i < 4; ++i) { sa_ = max(sa_, (unsigned)__builtin_amdgcn_readlane(s16, i)); sb_ = max(sb_, (unsigned)__builtin_amdgcn_readlane(s16, 4 + i));
              sv_ = max(sv_, max((unsigned)__builtin_amdgcn_readlane(s16, 8 + i), (unsigned)__builtin_amdgcn_readlane(s16, 12 + i))); } }
        const float kn = __uint_as_float(sa_) + (ka != kb ? __uint_as_float(sb_) : 0.f);
        int fasti = (kn * qn * 1.05f <= SAFE_T2 && __uint_as_float(sv_) <= SAFE_V2) ? 1 : 0;
        const bool fast = __builtin_amdgcn_readfirstlane(fasti) != 0;
        float m[4] = {0.f, 0.f, 0.f, 0.f};
        if (__builtin_expect(fast, 1)) {
            f32x16 sa, sb = {};
            u32x4 plo = {}, phi = {};
            const f32x16 zero = {};
            sa = MFMA32(BF(kf[0][0]), qf[0][0], zero); sa = MFMA32(BF(kf[0][1]), qf[0][1], sa);
            SB();
#define EX(S, i) S[i] = __builtin_amdgcn_exp2f(S[i])
#define STEP(SC, SN, B, t, X1, X2, X3, X4) do { \
            constexpr int tn = ((t) + 1) & 3, bn = (t) == 3 ? ((B) + 1) & 3 : (B), tp = ((t) + 3) & 3, bp = (t) == 0 ? ((B) + 3) & 3 : (B); \
            o[tp] = MFMA32(BF(vf[bp][0]), BF(plo), o[tp]); SB(); X1; SB(); \
            phi[2] = cvt2(SN[12], SN[13]); phi[3] = cvt2(SN[14], SN[15]); PIN(phi); SB(); EX(SC, 0); EX(SC, 1); EX(SC, 2); EX(SC, 3); PIN(SC); SB(); \
            SN = MFMA32(BF(kf[bn][0]), qf[tn][0], zero); SB(); X2; SB(); \
            plo[0] = cvt2(SC[0], SC[1]); plo[1] = cvt2(SC[2], SC[3]); PIN(plo); SB(); EX(SC, 4); EX(SC, 5); EX(SC, 6); EX(SC, 7); PIN(SC); SB(); \
            SN = MFMA32(BF(kf[bn][1]), qf[tn][1], SN); SB(); X3; SB(); \
            plo[2] = cvt2(SC[4], SC[5]); plo[3] = cvt2(SC[6], SC[7]); PIN(plo); SB(); EX(SC, 8); EX(SC, 9); EX(SC, 10); EX(SC, 11); PIN(SC); SB(); \
            o[tp] = MFMA32(BF(vf[bp][1]), BF(phi), o[tp]); SB(); X4; SB(); \
            phi[0] = cvt2(SC[8], SC[9]); phi[1] = cvt2(SC[10], SC[11]); PIN(phi); SB(); EX(SC, 12); EX(SC, 13); EX(SC, 14); EX(SC, 15); PIN(SC); SB(); \
            } while (0)
#define NOP (void)0
#define PIN4(a, b, c_, d) asm volatile("" : "+v"(a), "+v"(b), "+v"(c_), "+v"(d))
#define PINS(x) asm volatile("" : "+s"(x))
#define BLOCK(B) \
            STEP(sa, sb, B, 0, vf[((B) + 3) & 3][0] = LDF(rv, 0, so), PIN4(vf[B][0], vf[B][1], kf[((B) + 1) & 3][0], kf[((B) + 1) & 3][1]), NOP, vf[((B) + 3) & 3][1] = LDF(rv, 1, so)); \
            STEP(sb, sa, B, 1, do { so += TREC; PINS(so); } while (0), NOP, NOP, NOP); \
            STEP(sa, sb, B, 2, NOP, kf[B][0] = LDF(rk, 0, so), kf[B][1] = LDF(rk, 1, so), NOP); \
            STEP(sb, sa, B, 3, NOP, NOP, NOP, NOP)
            unsigned so = 3 * TREC;
            for (int it = 0; it < 3; ++it) { BLOCK(0); BLOCK(1); BLOCK(2); BLOCK(3); }
            STEP(sa, sb, 0, 0, vf[3][0] = LDF(rv, 0, so), PIN4(vf[0][0], vf[0][1], kf[1][0], kf[1][1]), NOP, vf[3][1] = LDF(rv, 1, so));
            STEP(sb, sa, 0, 1, NOP, NOP, NOP, NOP); STEP(sa, sb, 0, 2, NOP, NOP, NOP, NOP); STEP(sb, sa, 0, 3, NOP, NOP, NOP, NOP);
            STEP(sa, sb, 1, 0, NOP, PIN4(vf[1][0], vf[1][1], kf[2][0], kf[2][1]), NOP, NOP); STEP(sb, sa, 1, 1, NOP, NOP, NOP, NOP); STEP(sa, sb, 1, 2, NOP, NOP, NOP, NOP); STEP(sb, sa, 1, 3, NOP, NOP, NOP, NOP);
            STEP(sa, sb, 2, 0, NOP, PIN4(vf[2][0], vf[2][1], kf[3][0], kf[3][1]), NOP, NOP); STEP(sb, sa, 2, 1, NOP, NOP, NOP, NOP); STEP(sa, sb, 2, 2, NOP, NOP, NOP, NOP); STEP(sb, sa, 2, 3, NOP, NOP, NOP, NOP);
            STEP(sa, sb, 3, 0, NOP, PIN4(vf[3][0], vf[3][1], kf[3][0], kf[3][1]), NOP, NOP); STEP(sb, sa, 3, 1, NOP, NOP, NOP, NOP);
            STEP(sa, sb, 3, 2, NOP, SENDQ(0, 0), SENDQ(0, 1), SENDQ(0, 2));
            STEP(sb, sa, 3, 3, SENDQ(0, 3), SENDQ(1, 0), SENDQ(1, 1), SENDQ(1, 2));
            o[3] = MFMA32(BF(vf[3][0]), BF(plo), o[3]); SB(); SENDQ(1, 3); SB();
            phi[2] = cvt2(sb[12], sb[13]); phi[3] = cvt2(sb[14], sb[15]); SB();
            SENDQ(2, 0); SB();
            o[3] = MFMA32(BF(vf[3][1]), BF(phi), o[3]); SB();
            SENDQ(2, 1); SENDQ(2, 2); SENDQ(2, 3);
        }
        PIN(fasti);
        if (__builtin_expect(__builtin_amdgcn_readfirstlane(fasti) == 0, 0)) {
#pragma unroll
            for (int t = 0; t < 4; ++t) m[t] = -INFINITY;
            for (int j = 0; j < 16; ++j) {
                kf[0][0] = LDF(rk, 0, j * TREC); kf[0][1] = LDF(rk, 1, j * TREC); vf[0][0] = LDF(rv, 0, j * TREC); vf[0][1] = LDF(rv, 1, j * TREC);
#pragma unroll
                for (int t = 0; t < 4; ++t) {
                    SB();
                    f32x16 s = {};
                    s = MFMA32(BF(kf[0][0]), qf[t][0], s);
                    s = MFMA32(BF(kf[0][1]), qf[t][1], s);
                    float bm = s[0];
#pragma unroll
                    for (int i = 1; i < 16; ++i) bm = fmaxf(bm, s[i]);
                    bm = fmaxf(bm, swap32(bm, hh));
                    const float mn = fmaxf(m[t], bm), al = __builtin_amdgcn_exp2f(m[t] - mn);
                    m[t] = mn;
                    o[t] *= al; PIN(o[t]); SB();
#pragma unroll
                    for (int i = 0; i < 16; ++i) s[i] = __builtin_amdgcn_exp2f(s[i] - mn);
                    PIN(s); SB();
                    o[t] = MFMA32(BF(vf[0][0]), BF(pack_lo(s)), o[t]);
                    o[t] = MFMA32(BF(vf[0][1]), BF(pack_hi(s)), o[t]);
                    PIN(o[t]);
                }
            }
            SEND(0, m[0]); SEND(1, m[1]); SEND(2, m[2]);
            o[3][13] = hh ? m[3] : o[3][13];
        }
        f32x16 acc = o[3];
        BAR();
        f32x16 p[3];
#pragma unroll
        for (int d = 0; d < 3; ++d)
#pragma unroll
            for (int g = 0; g < 4; ++g) { const f32x4 v = *(LAS const f32x4*)(lds + cmb_slot(w4 * 3 + d) + g * 1024 + lane * 16);
#pragma unroll
                for (int i = 0; i < 4; ++i) p[d][4 * g + i] = v[i]; }
        const bool plain = __all(!hh || (acc[13] == 0.f && p[0][13] == 0.f && p[1][13] == 0.f && p[2][13] == 0.f));
        if (__builtin_expect(plain, 1)) {
            acc = (acc + p[0]) + (p[1] + p[2]);
        } else {
            float mm[4] = {acc[13], p[0][13], p[1][13], p[2][13]};
#pragma unroll
            for (int d = 0; d < 4; ++d) { const float s = swap32(mm[d], hh); mm[d] = hh ? mm[d] : s; }
            const float M = fmaxf(fmaxf(mm[0], mm[1]), fmaxf(mm[2], mm[3]));
            acc *= __builtin_amdgcn_exp2f(mm[0] - M);
#pragma unroll
            for (int d = 0; d < 3; ++d) acc += p[d] * __builtin_amdgcn_exp2f(mm[d + 1] - M);
        }
        const float lsw = swap32(acc[12], hh);
        const float inv = __builtin_amdgcn_rcpf(hh ? acc[12] : lsw);
        acc *= inv;
        u32x4 lo = pack_lo(acc), hi = pack_hi(acc);
        if (hh) { hi[2] = 0u; hi[3] = 0u; }
#pragma unroll
        for (int i = 0; i < 4; ++i) { lo[i] = min(lo[i], SENT - 1u); hi[i] = min(hi[i], SENT - 1u); }
        __builtin_amdgcn_raw_buffer_store_b128(lo, rws, ooff, 0, SC1);
        __builtin_amdgcn_raw_buffer_store_b128(hi, rws, ooff + 1024, 0, SC1);
        MEMFENCE();
        if (lane == 0) (void)__hip_atomic_exchange((GAS u64*)(ws + WS_OFLAG + (T * 16 + h) * 8), tag, RLX_AGENT);
        __builtin_amdgcn_endpgm();
    }
}

extern "C" void kernel_launch(void* const* d_in, const int* in_sizes, int n_in, void* d_out, int out_size, void* d_ws, size_t ws_size, hipStream_t stream) {
    (void)hipFuncSetAttribute((const void*)k_mha, hipFuncAttributeMaxDynamicSharedMemorySize, LDS_BYTES);
    k_mha<<<256, 512, LDS_BYTES, stream>>>((const __bf16*)d_in[0], (const __bf16*)d_in[1], (const __bf16*)d_in[2], (char*)d_ws, (__bf16*)d_out);
}
```

```cpp
#include <hip/hip_runtime.h>
#include <hip/hip_bf16.h>
#include <stdint.h>

typedef __bf16 bf16x8 __attribute__((ext_vector_type(8)));
typedef __bf16 bf16x2 __attribute__((ext_vector_type(2)));
typedef float f32x16 __attribute__((ext_vector_type(16)));
typedef float f32x4 __attribute__((ext_vector_type(4)));
typedef float f32x2 __attribute__((ext_vector_type(2)));
typedef unsigned u32x4 __attribute__((ext_vector_type(4)));
typedef short s16x4 __attribute__((ext_vector_type(4)));
typedef unsigned long long u64;
#define LAS __attribute__((address_space(3)))
#define GAS __attribute__((address_space(1)))
#define DI __device__ __forceinline__
#define MFMA32(a, b, c) __builtin_amdgcn_mfma_f32_32x32x16_bf16((a), (b), (c), 0, 0, 0)
#define SB() __builtin_amdgcn_sched_barrier(0)
#define PIN(x) asm volatile("" : "+v"(x))
#define MEMFENCE() asm volatile("" ::: "memory")
#define RLX_AGENT __ATOMIC_RELAXED, __HIP_MEMORY_SCOPE_AGENT
#define RLX_WG __ATOMIC_RELAXED, __HIP_MEMORY_SCOPE_WORKGROUP
#define SC1 16
#define BF(x) __builtin_bit_cast(bf16x8, x)
#define U4(x) __builtin_bit_cast(u32x4, x)

constexpr int SEQ = 2048, DM = 448, DH = 28, N3 = 1344;
constexpr float QSCALE = 0.18898223650461362f * 1.4426950408889634f;
constexpr unsigned TREC = 61440, TR_V = 28672;
constexpr unsigned WS_KV = 4096, WS_O = 4u << 20, WS_FLAG = 6u << 20, WS_OFLAG = WS_FLAG + 32768, WS_EPOCH = WS_OFLAG + 8192, WS_END = WS_EPOCH + 256;
constexpr int LDS_W = 0, LDS_X = 57344, XSLOT = 16384, LDS_WQ = LDS_X + XSLOT, LDS_WO = LDS_X, LDS_Q = 40960, LDS_MISC = 157696, LDS_XY = LDS_WO + 57344, LDS_BYTES = LDS_MISC + 64 + 256;
DI int cmb_slot(int k) { return 4096 * (k < 10 ? k : k + 2); }
constexpr float SAFE_T2 = 90.f * 90.f;
constexpr float SAFE_V2 = 1.0e12f;
constexpr int CODE_BYTES = 29696;
constexpr unsigned SPIN_CAP = 1u << 18;
constexpr unsigned SENT = 0xFFFFFFFFu, OOB = 0xFFFFFF00u;

DI bf16x8 tr_frag(LAS const char* p0, LAS const char* p1) {
    s16x4 a = __builtin_bit_cast(s16x4, __builtin_amdgcn_ds_read_tr16_b64_v4i16((LAS s16x4*)p0));
    s16x4 b = __builtin_bit_cast(s16x4, __builtin_amdgcn_ds_read_tr16_b64_v4i16((LAS s16x4*)p1));
    return __builtin_bit_cast(bf16x8, __builtin_shufflevector(a, b, 0, 1, 2, 3, 4, 5, 6, 7));
}
DI u32x4 pack_lo(const f32x16& c) { return U4(__builtin_convertvector(__builtin_shufflevector(c, c, 0, 1, 2, 3, 4, 5, 6, 7), bf16x8)); }
DI u32x4 pack_hi(const f32x16& c) { return U4(__builtin_convertvector(__builtin_shufflevector(c, c, 8, 9, 10, 11, 12, 13, 14, 15), bf16x8)); }
DI unsigned cvt2(float a, float b) { return __builtin_bit_cast(unsigned, __builtin_convertvector((f32x2){a, b}, bf16x2)); }
DI float swap32(float v, bool hi) {
    auto r = __builtin_amdgcn_permlane32_swap(__float_as_uint(v), __float_as_uint(v), false, false);
    return hi ? __uint_as_float(r[0]) : __uint_as_float(r[1]);
}
DI u32x4 piece(u32x4 v) {
    auto a = __builtin_amdgcn_permlane32_swap(v[0], v[2], false, false), b = __builtin_amdgcn_permlane32_swap(v[1], v[3], false, false);
    return (u32x4){a[0], b[0], a[1], b[1]};
}
DI float sumsq(u32x4 v) {
    float s = 0.f;
#pragma unroll
    for (int i = 0; i < 4; ++i) { const unsigned u = v[i]; const bf16x2 p = __builtin_bit_cast(bf16x2, u); s = __builtin_amdgcn_fdot2_f32_bf16(p, p, s, false); }
    return s;
}
#define WAIT_FLAGS(res, ta, tb, off, tag_, MASK, SH) do { u64 t_; unsigned n_; \
    asm volatile("s_mov_b64 exec, " MASK "\n\ts_mov_b32 %4, %8\n\t" \
                 "v_mov_b64 %1, 0\n\tglobal_atomic_add_x2 %1, %5, %1, %6 sc0\n\ts_sleep 6\n" \
                 "1:\n\tv_mov_b64 %2, 0\n\tglobal_atomic_add_x2 %2, %5, %2, %6 sc0\n\ts_waitcnt vmcnt(1)\n\t" \
                 "v_lshrrev_b64 %3, " SH ", %1\n\tv_mov_b64 %0, %1\n\tv_cmp_ne_u64 vcc, %7, %3\n\ts_cbranch_vccz 2f\n\t" \
                 "v_mov_b64 %1, 0\n\tglobal_atomic_add_x2 %1, %5, %1, %6 sc0\n\ts_waitcnt vmcnt(1)\n\t" \
                 "v_lshrrev_b64 %3, " SH ", %2\n\tv_mov_b64 %0, %2\n\tv_cmp_ne_u64 vcc, %7, %3\n\ts_cbranch_vccz 2f\n\t" \
                 "s_sub_u32 %4, %4, 1\n\ts_cbranch_scc0 1b\n" \
                 "2:\n\ts_mov_b64 exec, -1" \
                 : "=&v"(res), "=&v"(ta), "=&v"(tb), "=&v"(t_), "=&s"(n_) : "v"(off), "s"(ws), "s"(tag_), "i"(SPIN_CAP) : "memory", "vcc", "scc"); } while (0)
DI unsigned pos_bits(float v) { return (v == v) ? __float_as_uint(v) : 0x7F800000u; }
DI void dma16(u32x4 rsrc, unsigned voff, unsigned soff, unsigned lds_dst) {
    unsigned keep;
    asm volatile("s_mov_b32 %0, m0\n\ts_mov_b32 m0, %4\n\ts_nop 0\n\tbuffer_load_dwordx4 %1, %2, %3 offen lds\n\ts_mov_b32 m0, %0"
                 : "=&s"(keep) : "v"(voff), "s"(rsrc), "s"(soff), "s"(lds_dst) : "memory");
}
DI u32x4 mk_rsrc(const void* p, unsigned bytes) { const u64 a = (u64)p; return (u32x4){(unsigned)a, (unsigned)(a >> 32) & 0xffffu, bytes, 0x00020000u}; }
#define WAITV(n) asm volatile("s_waitcnt vmcnt(%c0)" :: "i"(n) : "memory")
#define BAR() asm volatile("s_waitcnt lgkmcnt(0)\n\ts_barrier" ::: "memory")
DI void lds_max(unsigned addr, unsigned v) { asm volatile("ds_max_u32 %0, %1" :: "v"(addr), "v"(v) : "memory"); }
DI void wait4(LAS unsigned* cnt) {
    for (unsigned n = 0; __builtin_amdgcn_readfirstlane(__hip_atomic_load(cnt, RLX_WG)) < 4u && n < (SPIN_CAP << 6); ++n) __builtin_amdgcn_s_sleep(1);
    MEMFENCE();
}
DI void sync4(LAS unsigned* cnt, int lane) {
    asm volatile("s_waitcnt lgkmcnt(0)" ::: "memory");
    if (lane == 0) __hip_atomic_fetch_add(cnt, 1u, RLX_WG);
    wait4(cnt);
}

__global__ __launch_bounds__(512, 2) void k_mha(const __bf16* x, const __bf16* w, const __bf16* wo, char* ws, __bf16* out) {
    extern __shared__ __attribute__((aligned(16))) char lds_[];
    { u64 pc_; unsigned keep; const unsigned po = min((blockIdx.x >> 3) * 1024u + (threadIdx.x >> 5) * 64u, (unsigned)CODE_BYTES);
      asm volatile("s_mov_b32 %1, m0\n\ts_mov_b32 m0, %3\n\ts_getpc_b64 %0\n\tglobal_load_lds_dword %2, %0\n\ts_mov_b32 m0, %1"
                   : "=&s"(pc_), "=&s"(keep) : "v"(po), "s"((unsigned)(uintptr_t)(LAS char*)lds_ + LDS_MISC + 64) : "memory"); }
    LAS char* lds = (LAS char*)lds_;
    LAS unsigned* misc = (LAS unsigned*)(lds + LDS_MISC);
    const unsigned lds0 = (unsigned)(uintptr_t)lds;
    const int bid = blockIdx.x;
    const int h = (bid & 1) * 8 + ((bid >> 3) & 7), r = ((bid >> 1) & 3) * 4 + (bid >> 6);
    const int lane = threadIdx.x & 63, wid = __builtin_amdgcn_readfirstlane(threadIdx.x >> 6), w4 = wid & 3;
    const int c = lane & 31; const bool hh = lane >= 32;
    const int T = 4 * r + w4;
    const bool roleA = wid < 4;
    const unsigned vx = (lane >> 3) * 896 + (((lane & 7) ^ (lane >> 4)) * 16), vx1 = vx ^ 64;
    const unsigned vw = (lane >> 3) * (N3 * 2) + 896 + (h < 14 ? h : 0) * 128 + (((lane & 7) ^ (((lane >> 4) & 1) << 2)) * 16);
    const unsigned xs0 = 32 * T * 896;
    unsigned ve = roleA ? vx : vw, vo = roleA ? vx1 : vw;
    unsigned s0 = roleA ? xs0 : w4 * 8 * N3 * 2, ds = roleA ? 7168 : 32 * N3 * 2, l0 = lds0 + (roleA ? LDS_X + w4 * 4096 : LDS_W + w4 * 1024), dl = roleA ? 1024 : 4096;
    asm volatile("" : "+v"(ve), "+v"(vo), "+s"(s0), "+s"(ds), "+s"(l0), "+s"(dl));
    SB();
    asm volatile("" : "+s"(x), "+s"(w), "+s"(wo), "+s"(ws), "+s"(out));
    const u32x4 rx = mk_rsrc(x, SEQ * DM * 2), rwt = mk_rsrc(w, h < 14 ? DM * N3 * 2 : 0);
    {
        const u32x4 r0 = roleA ? rx : rwt;
#pragma unroll
        for (int k = 0; k < 4; ++k) dma16(r0, (k & 1) ? vo : ve, s0 + k * ds, l0 + k * dl);
    }
    SB();
    const __amdgpu_buffer_rsrc_t rws = __builtin_amdgcn_make_buffer_rsrc(ws, 0, WS_END, 0x00020000);
    const int blk = (lane >> 4) & 1, q4 = (lane & 15) >> 2, p4 = lane & 3, h8 = hh ? 8 : 0;
    const unsigned xa0 = LDS_X + w4 * 4096 + c * 128 + (((hh ? 1 : 0) ^ ((c >> 1) & 7)) * 16);

    if (!roleA) {
        asm volatile("; role B" ::: "memory");
        const u32x4 rw = mk_rsrc(w, DM * N3 * 2);
#define LDW(p) do { dma16(rwt, vw, (8 * (p) + w4) * 8 * N3 * 2, lds0 + LDS_W + (8 * (p) + w4) * 1024); dma16(rwt, vw, (8 * (p) + 4 + w4) * 8 * N3 * 2, lds0 + LDS_W + (8 * (p) + 4 + w4) * 1024); } while (0)
        LDW(2);
        const unsigned vq = ((lane >> 2) * N3 + ((7 * h) >> 1) * 8 + (lane & 3) * 8) * 2;
        bf16x8 xf[28];
#define RDX(p) do { _Pragma("unroll") for (int s = 0; s < 4; ++s) xf[4 * (p) + s] = *(LAS const bf16x8*)(lds + ((p) % 6) * XSLOT + (xa0 ^ (32 * s))); } while (0)
#define LDQ(p) dma16(rw, vq, ((p) * 64 + 16 * w4) * N3 * 2, lds0 + LDS_WQ + (4 * (p) + w4) * 1024)
        WAITV(4); BAR(); LDW(3); LDW(4); RDX(0);
        WAITV(6); BAR(); LDW(5); LDW(6); RDX(1);
        WAITV(8); BAR(); RDX(2);
        WAITV(6); BAR(); LDQ(0); LDQ(1); LDQ(2); RDX(3);
        WAITV(7); BAR(); LDQ(3); LDQ(4); RDX(4);
        WAITV(7); BAR(); LDQ(5); LDQ(6); RDX(5);
        WAITV(7); BAR(); RDX(6);
        WAITV(0); sync4(misc + 3, lane);
        {
            LAS const char* qb = lds + LDS_WQ + (h8 + q4) * 64 + 32 * blk + 8 * p4;
            f32x16 aq = {};
            bf16x8 wq[28];
#pragma unroll
            for (int s = 0; s < 6; ++s) wq[s] = tr_frag(qb + 1024 * s, qb + 1024 * s + 256);
            SB();
#pragma unroll
            for (int s = 0; s < 28; ++s) { aq = MFMA32(wq[s], xf[s], aq); SB(); if (s + 6 < 28) wq[s + 6] = tr_frag(qb + 1024 * (s + 6), qb + 1024 * (s + 6) + 256); SB(); }
#pragma unroll
            for (int i = 0; i < 16; ++i) aq[i] *= QSCALE;
#pragma unroll
            for (int i = 0; i < 4; ++i) { aq[12 + i] = (hh && !(h & 1)) ? 0.f : aq[12 + i]; aq[i] = (!hh && (h & 1)) ? 0.f : aq[i]; }
            *(LAS u32x4*)(lds + LDS_Q + w4 * 2048 + lane * 16) = piece(pack_lo(aq));
            *(LAS u32x4*)(lds + LDS_Q + w4 * 2048 + 1024 + lane * 16) = piece(pack_hi(aq));
            float qn = 0.f;
#pragma unroll
            for (int i = 0; i < 16; ++i) qn = fmaf(aq[i], aq[i], qn);
            qn += swap32(qn, hh);
            lds_max(lds0 + LDS_MISC + 8, pos_bits(qn));
        }
        sync4(misc + 7, lane);
        if (h >= 14) __builtin_amdgcn_endpgm();
        const int c3 = h >= 7 ? h - 7 : h, kh = w4 >> 1, T3 = 4 * r + (h >= 7 ? 2 : 0) + (w4 & 1);
        wait4(misc + 8);
        {
            const u32x4 rwo = mk_rsrc(wo, DM * DM * 2);
            const unsigned vo = (lane >> 3) * (DM * 2) + c3 * 128 + (((lane & 7) ^ (((lane >> 4) & 1) << 2)) * 16);
#pragma unroll
            for (int i = 0; i < 14; ++i) dma16(rwo, vo, (w4 + 4 * i) * 8 * DM * 2, lds0 + LDS_WO + (w4 + 4 * i) * 1024);
        }
        const u64 tag = (u64)misc[5] | ((u64)misc[6] << 32);
        const int sw = (q4 >> 1) & 1;
        unsigned b1 = LDS_WO + (kh * 8 * DH + (hh ? 4 : 0) + q4) * 128 + 64 * (sw ^ kh) + 32 * blk + 8 * p4;
        unsigned b3 = b1 + (hh ? 20 : 24) * 128;
        unsigned oin = WS_O + (T3 * 16 + 8 * kh) * 2048 + lane * 16;
        unsigned fpo = WS_OFLAG + (T3 * 16 + 8 * kh + (lane & 7)) * 8;
        LAS char* exs = lds + LDS_XY + ((w4 & 1) * 2 + (kh ^ 1)) * 4096 + lane * 16;
        LAS const char* exr = lds + LDS_XY + ((w4 & 1) * 2 + kh) * 4096 + lane * 16;
        GAS __bf16* orow = (GAS __bf16*)out + (size_t)(32 * T3 + c) * DM + c3 * 64 + kh * 32 + h8;
        PIN(orow); PIN(fpo); PIN(oin); PIN(b1); PIN(b3); PIN(exs); PIN(exr);
        if (bid == 0 && w4 == 0) {
            GAS u64* sp = (GAS u64*)(ws + WS_FLAG + (((lane >> 3) & 3) * 256 + 14) * 32 + (lane & 7) * 8);
            for (unsigned spins = 0;; ++spins) { if (__all((__hip_atomic_load(sp, RLX_AGENT) >> 16) == tag) || spins > SPIN_CAP) break; __builtin_amdgcn_s_sleep(8); }
        }
        f32x16 pa[2] = {};
        bf16x8 wf[8][2][2];
        WAITV(0); BAR();
        {
#pragma unroll
            for (int i = 0; i < 8; ++i)
#pragma unroll
                for (int a = 0; a < 2; ++a) {
                    LAS const char *p1 = lds + (b1 ^ (64 * a)), *p3 = lds + (b3 ^ (64 * a));
                    wf[i][0][a] = tr_frag(p1 + i * 3584, p1 + i * 3584 + 1024); wf[i][1][a] = tr_frag(p1 + i * 3584 + 2048, p3 + i * 3584);
                    PIN(wf[i][0][a]); PIN(wf[i][1][a]); }
            u64 fv, fa, fb;
            WAIT_FLAGS(fv, fa, fb, fpo, tag, "0xff", "0");
            for (unsigned spins = 0;; ++spins) {
                MEMFENCE();
                u32x4 of[8][2];
#pragma unroll
                for (int i = 0; i < 8; ++i) { of[i][0] = __builtin_amdgcn_raw_buffer_load_b128(rws, oin, i * 2048, SC1); SB(); of[i][1] = __builtin_amdgcn_raw_buffer_load_b128(rws, oin + 1024, i * 2048, SC1); SB(); }
                asm volatile("s_waitcnt vmcnt(16)" : "+v"(fa), "+v"(fb) :: "memory");
                unsigned mx = 0;
                pa[0] = (f32x16){}; pa[1] = (f32x16){};
#pragma unroll
                for (int i = 0; i < 8; ++i)
#pragma unroll
                    for (int k = 0; k < 2; ++k) {
                        pa[1] = MFMA32(wf[i][k][1], BF(of[i][k]), pa[1]); SB();
                        mx = max(max(mx, of[i][k][0]), of[i][k][1]); mx = max(max(mx, of[i][k][2]), of[i][k][3]); PIN(mx); SB();
                        pa[0] = MFMA32(wf[i][k][0], BF(of[i][k]), pa[0]); SB();
                    }
                if (!__any(mx == SENT) || spins > SPIN_CAP) break;
            }
        }
        {
#pragma unroll
            for (int g = 0; g < 4; ++g) *(LAS f32x4*)(exs + g * 1024) = (f32x4){pa[1][4 * g], pa[1][4 * g + 1], pa[1][4 * g + 2], pa[1][4 * g + 3]};
            BAR();
#pragma unroll
            for (int g = 0; g < 4; ++g) { const f32x4 v = *(LAS const f32x4*)(exr + g * 1024);
#pragma unroll
                for (int i = 0; i < 4; ++i) pa[0][4 * g + i] += v[i]; }
            *(GAS u32x4*)(orow) = piece(pack_lo(pa[0]));
            *(GAS u32x4*)(orow + 16) = piece(pack_hi(pa[0]));
        }
        if (bid == 0 && threadIdx.x == 256) __hip_atomic_store((GAS u64*)(ws + WS_EPOCH), tag, RLX_AGENT);
        __builtin_amdgcn_endpgm();
    } else {
#define LDX(p) do { const unsigned sl = lds0 + LDS_X + ((p) % 6) * XSLOT + w4 * 4096; \
            dma16(rx, vx, xs0 + (p) * 128, sl); dma16(rx, vx1, xs0 + (p) * 128 + 8 * 896, sl + 1024); dma16(rx, vx, xs0 + (p) * 128 + 16 * 896, sl + 2048); dma16(rx, vx1, xs0 + (p) * 128 + 24 * 896, sl + 3072); } while (0)
        LDX(1); LDX(2);
        SB();
        const u64 epoch = __hip_atomic_load((GAS u64*)(ws + WS_EPOCH), RLX_AGENT);
        if (threadIdx.x < 12) misc[threadIdx.x] = 0u;
        unsigned ooff = WS_O + (T * 16 + h) * 2048 + lane * 16;
        PIN(ooff);
#define AUX() do { \
        __builtin_amdgcn_raw_buffer_store_b128((u32x4){SENT, SENT, SENT, SENT}, rws, ooff, 0, SC1); \
        __builtin_amdgcn_raw_buffer_store_b128((u32x4){SENT, SENT, SENT, SENT}, rws, ooff + 1024, 0, SC1); } while (0)
        const bool isk = h < 7, isv = h >= 7 && h < 14;
        const int ct = isk ? h : h - 7;
        unsigned dst0, dst1, dstp;
        { const int c0 = 64 * ct + c, h0 = (c0 * 293) >> 13, c1 = c0 + 32, h1 = (c1 * 293) >> 13;
          dst0 = WS_KV + T * TREC + (isk ? ct * 4096 + lane * 16 : TR_V + h0 * 2048 + (c0 - DH * h0 + (hh ? 32 : 0)) * 16);
          dst1 = WS_KV + T * TREC + TR_V + h1 * 2048 + (c1 - DH * h1 + (hh ? 32 : 0)) * 16;
          const int hp = (64 * ct) / DH + (lane >> 4);
          dstp = (DH * hp + DH - 1 < 64 * ct + 64) ? WS_KV + T * TREC + TR_V + hp * 2048 + ((lane >> 2) & 3) * 512 + (DH + (lane & 3)) * 16 : OOB;
          PIN(dst0); PIN(dst1); PIN(dstp); }
        const int sw = (q4 >> 1) & 1;
        const unsigned wa0 = LDS_W + (h8 + q4) * 128 + 64 * sw + 32 * blk + 8 * p4, wa1 = wa0 ^ 64;
        f32x16 a0 = {}, a1 = {};
        bf16x8 xf_[2][4], w0_[2][4], w1_[2][4];
#define XW(p) ((p) == 0 ? 9 : (p) == 1 ? 13 : (p) == 2 ? 19 : 4 * (6 - (p)) + 2)
#define P1RD1(p, s) do { xf_[(p) & 1][s] = *(LAS const bf16x8*)(lds + ((p) % 6) * XSLOT + (xa0 ^ (32 * (s)))); \
            w0_[(p) & 1][s] = tr_frag(lds + wa0 + 8192 * (p) + 2048 * (s), lds + wa0 + 8192 * (p) + 2048 * (s) + 512); } while (0)
#define P1RD2(p, s) do { w1_[(p) & 1][s] = tr_frag(lds + wa1 + 8192 * (p) + 2048 * (s), lds + wa1 + 8192 * (p) + 2048 * (s) + 512); } while (0)
#define P1M(acc, wv, p, s, KT) do { if (KT) acc = MFMA32(wv[(p) & 1][s], xf_[(p) & 1][s], acc); else acc = MFMA32(xf_[(p) & 1][s], wv[(p) & 1][s], acc); } while (0)
#define P1X(p, s) (xf_[(p) & 1][s] = *(LAS const bf16x8*)(lds + ((p) % 6) * XSLOT + (xa0 ^ (32 * (s)))))
#define P1W(wv, wa, p, s) (wv[(p) & 1][s] = tr_frag(lds + wa + 8192 * (p) + 2048 * (s), lds + wa + 8192 * (p) + 2048 * (s) + 512))
#define P1MM(acc, wv, p, s, KT) do { if ((p) > 0) { P1M(acc, wv, (p) - 1, s, KT); PIN(acc); } SB(); } while (0)
#define P1STEP(p, KT) do { WAITV(XW(p)); BAR(); SB(); if (KT < 2) { \
            P1MM(a0, w0_, p, 0, KT); P1X(p, 0); P1W(w0_, wa0, p, 0); SB(); P1MM(a1, w1_, p, 0, KT); P1W(w1_, wa1, p, 0); P1X(p, 1); SB(); \
            P1MM(a0, w0_, p, 1, KT); P1W(w0_, wa0, p, 1); P1W(w1_, wa1, p, 1); SB(); P1MM(a1, w1_, p, 1, KT); P1X(p, 2); P1W(w0_, wa0, p, 2); SB(); \
            P1MM(a0, w0_, p, 2, KT); P1W(w1_, wa1, p, 2); P1X(p, 3); SB(); P1MM(a1, w1_, p, 2, KT); P1W(w0_, wa0, p, 3); P1W(w1_, wa1, p, 3); SB(); \
            P1MM(a0, w0_, p, 3, KT); } if ((p) <= 1) { LDX(2 * (p) + 3); LDX(2 * (p) + 4); } if ((p) == 1) AUX(); SB(); if (KT < 2) P1MM(a1, w1_, p, 3, KT); } while (0)
#define P1L(acc, wv, s, KT) do { P1M(acc, wv, 6, s, KT); PIN(acc); SB(); } while (0)
#define P1LAST(KT, F, d0, d1) do { P1L(a0, w0_, 0, KT); P1L(a0, w0_, 1, KT); P1L(a0, w0_, 2, KT); P1L(a0, w0_, 3, KT); P1L(a1, w1_, 0, KT); P1L(a1, w1_, 1, KT); \
            k0 = F(pack_lo(a0)); __builtin_amdgcn_raw_buffer_store_b128(k0, rws, d0, 0, SC1); SB(); P1L(a1, w1_, 2, KT); \
            k1 = F(pack_hi(a0)); __builtin_amdgcn_raw_buffer_store_b128(k1, rws, (d0) + 1024, 0, SC1); SB(); P1L(a1, w1_, 3, KT); \
            stat = sumsq(k0) + sumsq(k1); PIN(stat); SB(); \
            k2 = F(pack_lo(a1)); __builtin_amdgcn_raw_buffer_store_b128(k2, rws, d1, 0, SC1); k3 = F(pack_hi(a1)); __builtin_amdgcn_raw_buffer_store_b128(k3, rws, (d1) + 1024, 0, SC1); SB(); \
            stat += sumsq(k2) + sumsq(k3); } while (0)
#define P1ALL(KT) do { P1STEP(0, KT); P1STEP(1, KT); P1STEP(2, KT); P1STEP(3, KT); P1STEP(4, KT); P1STEP(5, KT); P1STEP(6, KT); } while (0)
        float stat = 0.f;
        u32x4 k0, k1, k2, k3;
        if (isk) {
            P1ALL(1); P1LAST(1, piece, dst0, dst0 + 2048);
            stat += swap32(stat, hh);
        } else if (isv) {
            const unsigned one = (lane & 3) == 0 ? 0x3F803F80u : 0u;
            P1ALL(0);
            __builtin_amdgcn_raw_buffer_store_b128((u32x4){one, one, one, one}, rws, dstp, 0, SC1);
            P1LAST(0, , dst0, dst1);
        } else {
            P1ALL(2);
        }
        lds_max(lds0 + LDS_MISC + 4, pos_bits(stat));
        GAS u64* fo = (GAS u64*)(ws + WS_FLAG + (((r >> 2) * 16 + (lane & 15)) * 16 + h) * 32 + (r & 3) * 8);
        PIN(fo);
        asm volatile("s_waitcnt vmcnt(0) lgkmcnt(0)" ::: "memory");
        const u64 tag = (((u64)__builtin_amdgcn_readfirstlane((unsigned)(epoch >> 32)) << 32 | (unsigned)__builtin_amdgcn_readfirstlane((unsigned)epoch)) + 1ull) & 0xFFFFFFFFFFFFull;
        {
            if (threadIdx.x == 0) { misc[5] = (unsigned)tag; misc[6] = (unsigned)(tag >> 32); }
            unsigned arr = 0;
            if (lane == 0) arr = __hip_atomic_fetch_add(misc, 1u, RLX_WG);
            MEMFENCE();
            const unsigned sb = (__hip_atomic_load(misc + 1, RLX_WG) + 0xFFFFu) >> 16;
            if (__builtin_amdgcn_readfirstlane(arr) == 3u && lane < 16) (void)__hip_atomic_exchange(fo, tag << 16 | sb, RLX_AGENT);
        }
        const int pc0 = (7 * h) >> 1, ka = pc0 >> 3, kb = (pc0 + 3) >> 3, va = (DH * h) >> 6, vb = (DH * h + DH - 1) >> 6;
        const int l4 = (lane >> 2) & 3;
        const unsigned fpo = WS_FLAG + ((w4 * 16 + r) * 16 + (l4 == 0 ? ka : l4 == 1 ? kb : l4 == 2 ? 7 + va : 7 + vb)) * 32 + (lane & 3) * 8;
        u64 fv, fa, fb;
        SB();
        f32x16 o[4] = {};
        PIN(o[0]); PIN(o[1]); PIN(o[2]); PIN(o[3]);
        wait4(misc); wait4(misc + 7);
        bf16x8 qf[4][2];
#pragma unroll
        for (int t = 0; t < 4; ++t) { const int tg = (w4 + 1 + t) & 3; qf[t][0] = *(LAS const bf16x8*)(lds + LDS_Q + tg * 2048 + lane * 16); qf[t][1] = *(LAS const bf16x8*)(lds + LDS_Q + tg * 2048 + 1024 + lane * 16); }
        const float qn = __uint_as_float(misc[2]);
        LAS char* cmb[3];
#pragma unroll
        for (int t = 0; t < 3; ++t) cmb[t] = lds + cmb_slot(((w4 + 1 + t) & 3) * 3 + 2 - t) + lane * 16;
#define SENDQ(t, g) (*(LAS f32x4*)(cmb[t] + (g) * 1024) = (f32x4){o[t][4 * (g)], o[t][4 * (g) + 1], o[t][4 * (g) + 2], o[t][4 * (g) + 3]})
#define SEND(t, mv) do { o[t][13] = hh ? (mv) : o[t][13]; SENDQ(t, 0); SENDQ(t, 1); SENDQ(t, 2); SENDQ(t, 3); } while (0)
        WAIT_FLAGS(fv, fa, fb, fpo, tag, "0xffff", "16");
        MEMFENCE();
        const __amdgpu_buffer_rsrc_t rk = __builtin_amdgcn_make_buffer_rsrc(ws + WS_KV + 16 * w4 * TREC + pc0 * 512, 16, 16 * TREC, 1 << 23);
        const __amdgpu_buffer_rsrc_t rv = __builtin_amdgcn_make_buffer_rsrc(ws + WS_KV + 16 * w4 * TREC + TR_V + h * 2048, 16, 16 * TREC, 1 << 23);
#define LDF(rs_, f_, so_) __builtin_amdgcn_raw_buffer_load_b128(rs_, (f_) * 1024, so_, SC1)
        u32x4 kf[4][2], vf[4][2];
#pragma unroll
        for (int b = 0; b < 4; ++b) { kf[b][0] = LDF(rk, 0, b * TREC); SB(); kf[b][1] = LDF(rk, 1, b * TREC); SB();
            if (b < 3) { vf[b][0] = LDF(rv, 0, b * TREC); SB(); vf[b][1] = LDF(rv, 1, b * TREC); SB(); } }
        vf[3][0] = (u32x4){0u, 0u, 0u, 0u}; vf[3][1] = vf[3][0];
        if (lane == 0) __hip_atomic_fetch_add(misc + 8, 1u, RLX_WG);
        SB();
        asm volatile("s_waitcnt vmcnt(14)" : "+v"(fa), "+v"(fb) :: "memory");
        unsigned sa_ = 0, sb_ = 0, sv_ = 0;
        { const unsigned s16 = (unsigned)fv << 16;
#pragma unroll
          for (int i = 0; i < 4; ++i) { sa_ = max(sa_, (unsigned)__builtin_amdgcn_readlane(s16, i)); sb_ = max(sb_, (unsigned)__builtin_amdgcn_readlane(s16, 4 + i));
              sv_ = max(sv_, max((unsigned)__builtin_amdgcn_readlane(s16, 8 + i), (unsigned)__builtin_amdgcn_readlane(s16, 12 + i))); } }
        const float kn = __uint_as_float(sa_) + (ka != kb ? __uint_as_float(sb_) : 0.f);
        int fasti = (kn * qn * 1.05f <= SAFE_T2 && __uint_as_float(sv_) <= SAFE_V2) ? 1 : 0;
        const bool fast = __builtin_amdgcn_readfirstlane(fasti) != 0;
        float m[4] = {0.f, 0.f, 0.f, 0.f};
        if (__builtin_expect(fast, 1)) {
            f32x16 sa, sb = {};
            u32x4 plo = {}, phi = {};
            const f32x16 zero = {};
            sa = MFMA32(BF(kf[0][0]), qf[0][0], zero); sa = MFMA32(BF(kf[0][1]), qf[0][1], sa);
            SB();
#define EX(S, i) S[i] = __builtin_amdgcn_exp2f(S[i])
#define STEP(SC, SN, B, t, X1, X2, X3, X4) do { \
            constexpr int tn = ((t) + 1) & 3, bn = (t) == 3 ? ((B) + 1) & 3 : (B), tp = ((t) + 3) & 3, bp = (t) == 0 ? ((B) + 3) & 3 : (B); \
            o[tp] = MFMA32(BF(vf[bp][0]), BF(plo), o[tp]); SB(); X1; SB(); \
            phi[2] = cvt2(SN[12], SN[13]); phi[3] = cvt2(SN[14], SN[15]); PIN(phi); SB(); EX(SC, 0); EX(SC, 1); EX(SC, 2); EX(SC, 3); PIN(SC); SB(); \
            SN = MFMA32(BF(kf[bn][0]), qf[tn][0], zero); SB(); X2; SB(); \
            plo[0] = cvt2(SC[0], SC[1]); plo[1] = cvt2(SC[2], SC[3]); PIN(plo); SB(); EX(SC, 4); EX(SC, 5); EX(SC, 6); EX(SC, 7); PIN(SC); SB(); \
            SN = MFMA32(BF(kf[bn][1]), qf[tn][1], SN); SB(); X3; SB(); \
            plo[2] = cvt2(SC[4], SC[5]); plo[3] = cvt2(SC[6], SC[7]); PIN(plo); SB(); EX(SC, 8); EX(SC, 9); EX(SC, 10); EX(SC, 11); PIN(SC); SB(); \
            o[tp] = MFMA32(BF(vf[bp][1]), BF(phi), o[tp]); SB(); X4; SB(); \
            phi[0] = cvt2(SC[8], SC[9]); phi[1] = cvt2(SC[10], SC[11]); PIN(phi); SB(); EX(SC, 12); EX(SC, 13); EX(SC, 14); EX(SC, 15); PIN(SC); SB(); \
            } while (0)
#define NOP (void)0
#define PIN4(a, b, c_, d) asm volatile("" : "+v"(a), "+v"(b), "+v"(c_), "+v"(d))
#define PINS(x) asm volatile("" : "+s"(x))
#define BLOCK(B) \
            STEP(sa, sb, B, 0, vf[((B) + 3) & 3][0] = LDF(rv, 0, so), PIN4(vf[B][0], vf[B][1], kf[((B) + 1) & 3][0], kf[((B) + 1) & 3][1]), NOP, vf[((B) + 3) & 3][1] = LDF(rv, 1, so)); \
            STEP(sb, sa, B, 1, do { so += TREC; PINS(so); } while (0), NOP, NOP, NOP); \
            STEP(sa, sb, B, 2, NOP, kf[B][0] = LDF(rk, 0, so), kf[B][1] = LDF(rk, 1, so), NOP); \
            STEP(sb, sa, B, 3, NOP, NOP, NOP, NOP)
            unsigned so = 3 * TREC;
            for (int it = 0; it < 3; ++it) { BLOCK(0); BLOCK(1); BLOCK(2); BLOCK(3); }
            STEP(sa, sb, 0, 0, vf[3][0] = LDF(rv, 0, so), PIN4(vf[0][0], vf[0][1], kf[1][0], kf[1][1]), NOP, vf[3][1] = LDF(rv, 1, so));
            STEP(sb, sa, 0, 1, NOP, NOP, NOP, NOP); STEP(sa, sb, 0, 2, NOP, NOP, NOP, NOP); STEP(sb, sa, 0, 3, NOP, NOP, NOP, NOP);
            STEP(sa, sb, 1, 0, NOP, PIN4(vf[1][0], vf[1][1], kf[2][0], kf[2][1]), NOP, NOP); STEP(sb, sa, 1, 1, NOP, NOP, NOP, NOP); STEP(sa, sb, 1, 2, NOP, NOP, NOP, NOP); STEP(sb, sa, 1, 3, NOP, NOP, NOP, NOP);
            STEP(sa, sb, 2, 0, NOP, PIN4(vf[2][0], vf[2][1], kf[3][0], kf[3][1]), NOP, NOP); STEP(sb, sa, 2, 1, NOP, NOP, NOP, NOP); STEP(sa, sb, 2, 2, NOP, NOP, NOP, NOP); STEP(sb, sa, 2, 3, NOP, NOP, NOP, NOP);
            STEP(sa, sb, 3, 0, NOP, PIN4(vf[3][0], vf[3][1], kf[3][0], kf[3][1]), NOP, NOP); STEP(sb, sa, 3, 1, NOP, NOP, NOP, NOP);
            STEP(sa, sb, 3, 2, NOP, SENDQ(0, 0), SENDQ(0, 1), SENDQ(0, 2));
            STEP(sb, sa, 3, 3, SENDQ(0, 3), SENDQ(1, 0), SENDQ(1, 1), SENDQ(1, 2));
            o[3] = MFMA32(BF(vf[3][0]), BF(plo), o[3]); SB(); SENDQ(1, 3); SB();
            phi[2] = cvt2(sb[12], sb[13]); phi[3] = cvt2(sb[14], sb[15]); SB();
            SENDQ(2, 0); SB();
            o[3] = MFMA32(BF(vf[3][1]), BF(phi), o[3]); SB();
            SENDQ(2, 1); SENDQ(2, 2); SENDQ(2, 3);
        }
        PIN(fasti);
        if (__builtin_expect(__builtin_amdgcn_readfirstlane(fasti) == 0, 0)) {
#pragma unroll
            for (int t = 0; t < 4; ++t) m[t] = -INFINITY;
            for (int j = 0; j < 16; ++j) {
                kf[0][0] = LDF(rk, 0, j * TREC); kf[0][1] = LDF(rk, 1, j * TREC); vf[0][0] = LDF(rv, 0, j * TREC); vf[0][1] = LDF(rv, 1, j * TREC);
#pragma unroll
                for (int t = 0; t < 4; ++t) {
                    SB();
                    f32x16 s = {};
                    s = MFMA32(BF(kf[0][0]), qf[t][0], s);
                    s = MFMA32(BF(kf[0][1]), qf[t][1], s);
                    float bm = s[0];
#pragma unroll
                    for (int i = 1; i < 16; ++i) bm = fmaxf(bm, s[i]);
                    bm = fmaxf(bm, swap32(bm, hh));
                    const float mn = fmaxf(m[t], bm), al = __builtin_amdgcn_exp2f(m[t] - mn);
                    m[t] = mn;
                    o[t] *= al; PIN(o[t]); SB();
#pragma unroll
                    for (int i = 0; i < 16; ++i) s[i] = __builtin_amdgcn_exp2f(s[i] - mn);
                    PIN(s); SB();
                    o[t] = MFMA32(BF(vf[0][0]), BF(pack_lo(s)), o[t]);
                    o[t] = MFMA32(BF(vf[0][1]), BF(pack_hi(s)), o[t]);
                    PIN(o[t]);
                }
            }
            SEND(0, m[0]); SEND(1, m[1]); SEND(2, m[2]);
            o[3][13] = hh ? m[3] : o[3][13];
        }
        f32x16 acc = o[3];
        BAR();
        f32x16 p[3];
#pragma unroll
        for (int d = 0; d < 3; ++d)
#pragma unroll
            for (int g = 0; g < 4; ++g) { const f32x4 v = *(LAS const f32x4*)(lds + cmb_slot(w4 * 3 + d) + g * 1024 + lane * 16);
#pragma unroll
                for (int i = 0; i < 4; ++i) p[d][4 * g + i] = v[i]; }
        const bool plain = __all(!hh || (acc[13] == 0.f && p[0][13] == 0.f && p[1][13] == 0.f && p[2][13] == 0.f));
        if (__builtin_expect(plain, 1)) {
            acc = (acc + p[0]) + (p[1] + p[2]);
        } else {
            float mm[4] = {acc[13], p[0][13], p[1][13], p[2][13]};
#pragma unroll
            for (int d = 0; d < 4; ++d) { const float s = swap32(mm[d], hh); mm[d] = hh ? mm[d] : s; }
            const float M = fmaxf(fmaxf(mm[0], mm[1]), fmaxf(mm[2], mm[3]));
            acc *= __builtin_amdgcn_exp2f(mm[0] - M);
#pragma unroll
            for (int d = 0; d < 3; ++d) acc += p[d] * __builtin_amdgcn_exp2f(mm[d + 1] - M);
        }
        const float lsw = swap32(acc[12], hh);
        const float inv = __builtin_amdgcn_rcpf(hh ? acc[12] : lsw);
        acc *= inv;
        u32x4 lo = pack_lo(acc), hi = pack_hi(acc);
        if (hh) { hi[2] = 0u; hi[3] = 0u; }
#pragma unroll
        for (int i = 0; i < 4; ++i) { lo[i] = min(lo[i], SENT - 1u); hi[i] = min(hi[i], SENT - 1u); }
        __builtin_amdgcn_raw_buffer_store_b128(lo, rws, ooff, 0, SC1);
        __builtin_amdgcn_raw_buffer_store_b128(hi, rws, ooff + 1024, 0, SC1);
        MEMFENCE();
        if (lane == 0) (void)__hip_atomic_exchange((GAS u64*)(ws + WS_OFLAG + (T * 16 + h) * 8), tag, RLX_AGENT);
        __builtin_amdgcn_endpgm();
    }
}

extern "C" void kernel_launch(void* const* d_in, const int* in_sizes, int n_in, void* d_out, int out_size, void* d_ws, size_t ws_size, hipStream_t stream) {
    (void)hipFuncSetAttribute((const void*)k_mha, hipFuncAttributeMaxDynamicSharedMemorySize, LDS_BYTES);
    k_mha<<<256, 512, LDS_BYTES, stream>>>((const __bf16*)d_in[0], (const __bf16*)d_in[1], (const __bf16*)d_in[2], (char*)d_ws, (__bf16*)d_out);
}
```

```cpp
#include <hip/hip_runtime.h>
#include <hip/hip_bf16.h>
#include <stdint.h>

typedef __bf16 bf16x8 __attribute__((ext_vector_type(8)));
typedef __bf16 bf16x2 __attribute__((ext_vector_type(2)));
typedef float f32x16 __attribute__((ext_vector_type(16)));
typedef float f32x4 __attribute__((ext_vector_type(4)));
typedef float f32x2 __attribute__((ext_vector_type(2)));
typedef unsigned u32x4 __attribute__((ext_vector_type(4)));
typedef short s16x4 __attribute__((ext_vector_type(4)));
typedef unsigned long long u64;
#define LAS __attribute__((address_space(3)))
#define GAS __attribute__((address_space(1)))
#define DI __device__ __forceinline__
#define MFMA32(a, b, c) __builtin_amdgcn_mfma_f32_32x32x16_bf16((a), (b), (c), 0, 0, 0)
#define SB() __builtin_amdgcn_sched_barrier(0)
#define PIN(x) asm volatile("" : "+v"(x))
#define MEMFENCE() asm volatile("" ::: "memory")
#define RLX_AGENT __ATOMIC_RELAXED, __HIP_MEMORY_SCOPE_AGENT
#define RLX_WG __ATOMIC_RELAXED, __HIP_MEMORY_SCOPE_WORKGROUP
#define SC1 16
#define BF(x) __builtin_bit_cast(bf16x8, x)
#define U4(x) __builtin_bit_cast(u32x4, x)

constexpr int SEQ = 2048, DM = 448, DH = 28, N3 = 1344;
constexpr float QSCALE = 0.18898223650461362f * 1.4426950408889634f;
constexpr unsigned TREC = 61440, TR_V = 28672;
constexpr unsigned WS_KV = 4096, WS_O = 4u << 20, WS_FLAG = 6u << 20, WS_OFLAG = WS_FLAG + 32768, WS_EPOCH = WS_OFLAG + 8192, WS_END = WS_EPOCH + 256;
constexpr int LDS_W = 0, LDS_X = 57344, XSLOT = 16384, LDS_WQ = LDS_X + XSLOT, LDS_WO = LDS_X, LDS_Q = 40960, LDS_MISC = 157696, LDS_XY = LDS_WO + 57344, LDS_BYTES = LDS_MISC + 64 + 256;
DI int cmb_slot(int k) { return 4096 * (k < 10 ? k : k + 2); }
constexpr float SAFE_T2 = 90.f * 90.f;
constexpr float SAFE_V2 = 1.0e12f;
#define OAH 6
#define BPRIO 2
constexpr int CODE_BYTES = 29696;
constexpr unsigned SPIN_CAP = 1u << 18;
constexpr unsigned SENT = 0xFFFFFFFFu, OOB = 0xFFFFFF00u;

DI bf16x8 tr_frag(LAS const char* p0, LAS const char* p1) {
    s16x4 a = __builtin_bit_cast(s16x4, __builtin_amdgcn_ds_read_tr16_b64_v4i16((LAS s16x4*)p0));
    s16x4 b = __builtin_bit_cast(s16x4, __builtin_amdgcn_ds_read_tr16_b64_v4i16((LAS s16x4*)p1));
    return __builtin_bit_cast(bf16x8, __builtin_shufflevector(a, b, 0, 1, 2, 3, 4, 5, 6, 7));
}
DI u32x4 pack_lo(const f32x16& c) { return U4(__builtin_convertvector(__builtin_shufflevector(c, c, 0, 1, 2, 3, 4, 5, 6, 7), bf16x8)); }
DI u32x4 pack_hi(const f32x16& c) { return U4(__builtin_convertvector(__builtin_shufflevector(c, c, 8, 9, 10, 11, 12, 13, 14, 15), bf16x8)); }
DI unsigned cvt2(float a, float b) { return __builtin_bit_cast(unsigned, __builtin_convertvector((f32x2){a, b}, bf16x2)); }
DI float swap32(float v, bool hi) {
    auto r = __builtin_amdgcn_permlane32_swap(__float_as_uint(v), __float_as_uint(v), false, false);
    return hi ? __uint_as_float(r[0]) : __uint_as_float(r[1]);
}
DI u32x4 piece(u32x4 v) {
    auto a = __builtin_amdgcn_permlane32_swap(v[0], v[2], false, false), b = __builtin_amdgcn_permlane32_swap(v[1], v[3], false, false);
    return (u32x4){a[0], b[0], a[1], b[1]};
}
DI float sumsq(u32x4 v) {
    float s = 0.f;
#pragma unroll
    for (int i = 0; i < 4; ++i) { const unsigned u = v[i]; const bf16x2 p = __builtin_bit_cast(bf16x2, u); s = __builtin_amdgcn_fdot2_f32_bf16(p, p, s, false); }
    return s;
}
#define WAIT_FLAGS(res, ta, tb, off, tag_, MASK, SH) do { u64 t_; unsigned n_; \
    asm volatile("s_mov_b64 exec, " MASK "\n\ts_mov_b32 %4, %8\n\t" \
                 "v_mov_b64 %1, 0\n\tglobal_atomic_add_x2 %1, %5, %1, %6 sc0\n\ts_sleep 6\n" \
                 "1:\n\tv_mov_b64 %2, 0\n\tglobal_atomic_add_x2 %2, %5, %2, %6 sc0\n\ts_waitcnt vmcnt(1)\n\t" \
                 "v_lshrrev_b64 %3, " SH ", %1\n\tv_mov_b64 %0, %1\n\tv_cmp_ne_u64 vcc, %7, %3\n\ts_cbranch_vccz 2f\n\t" \
                 "v_mov_b64 %1, 0\n\tglobal_atomic_add_x2 %1, %5, %1, %6 sc0\n\ts_waitcnt vmcnt(1)\n\t" \
                 "v_lshrrev_b64 %3, " SH ", %2\n\tv_mov_b64 %0, %2\n\tv_cmp_ne_u64 vcc, %7, %3\n\ts_cbranch_vccz 2f\n\t" \
                 "s_sub_u32 %4, %4, 1\n\ts_cbranch_scc0 1b\n" \
                 "2:\n\ts_mov_b64 exec, -1" \
                 : "=&v"(res), "=&v"(ta), "=&v"(tb), "=&v"(t_), "=&s"(n_) : "v"(off), "s"(ws), "s"(tag_), "i"(SPIN_CAP) : "memory", "vcc", "scc"); } while (0)
DI unsigned pos_bits(float v) { return (v == v) ? __float_as_uint(v) : 0x7F800000u; }
DI void dma16(u32x4 rsrc, unsigned voff, unsigned soff, unsigned lds_dst) {
    unsigned keep;
    asm volatile("s_mov_b32 %0, m0\n\ts_mov_b32 m0, %4\n\ts_nop 0\n\tbuffer_load_dwordx4 %1, %2, %3 offen lds\n\ts_mov_b32 m0, %0"
                 : "=&s"(keep) : "v"(voff), "s"(rsrc), "s"(soff), "s"(lds_dst) : "memory");
}
DI u32x4 mk_rsrc(const void* p, unsigned bytes) { const u64 a = (u64)p; return (u32x4){(unsigned)a, (unsigned)(a >> 32) & 0xffffu, bytes, 0x00020000u}; }
#define WAITV(n) asm volatile("s_waitcnt vmcnt(%c0)" :: "i"(n) : "memory")
#define BAR() asm volatile("s_waitcnt lgkmcnt(0)\n\ts_barrier" ::: "memory")
DI void lds_max(unsigned addr, unsigned v) { asm volatile("ds_max_u32 %0, %1" :: "v"(addr), "v"(v) : "memory"); }
DI void wait4(LAS unsigned* cnt) {
    for (unsigned n = 0; __builtin_amdgcn_readfirstlane(__hip_atomic_load(cnt, RLX_WG)) < 4u && n < (SPIN_CAP << 6); ++n) __builtin_amdgcn_s_sleep(1);
    MEMFENCE();
}
DI void sync4(LAS unsigned* cnt, int lane) {
    asm volatile("s_waitcnt lgkmcnt(0)" ::: "memory");
    if (lane == 0) __hip_atomic_fetch_add(cnt, 1u, RLX_WG);
    wait4(cnt);
}

__global__ __launch_bounds__(512, 2) void k_mha(const __bf16* x, const __bf16* w, const __bf16* wo, char* ws, __bf16* out) {
    extern __shared__ __attribute__((aligned(16))) char lds_[];
    { u64 pc_; unsigned keep; const unsigned po = min((blockIdx.x >> 3) * 1024u + (threadIdx.x >> 5) * 64u, (unsigned)CODE_BYTES);
      asm volatile("s_mov_b32 %1, m0\n\ts_mov_b32 m0, %3\n\ts_getpc_b64 %0\n\tglobal_load_lds_dword %2, %0\n\ts_mov_b32 m0, %1"
                   : "=&s"(pc_), "=&s"(keep) : "v"(po), "s"((unsigned)(uintptr_t)(LAS char*)lds_ + LDS_MISC + 64) : "memory"); }
    LAS char* lds = (LAS char*)lds_;
    LAS unsigned* misc = (LAS unsigned*)(lds + LDS_MISC);
    const unsigned lds0 = (unsigned)(uintptr_t)lds;
    const int bid = blockIdx.x;
    const int h = (bid & 1) * 8 + ((bid >> 3) & 7), r = ((bid >> 1) & 3) * 4 + (bid >> 6);
    const int lane = threadIdx.x & 63, wid = __builtin_amdgcn_readfirstlane(threadIdx.x >> 6), w4 = wid & 3;
    const int c = lane & 31; const bool hh = lane >= 32;
    const int T = 4 * r + w4;
    const bool roleA = wid < 4;
    const unsigned vx = (lane >> 3) * 896 + (((lane & 7) ^ (lane >> 4)) * 16), vx1 = vx ^ 64;
    const unsigned vw = (lane >> 3) * (N3 * 2) + 896 + (h < 14 ? h : 0) * 128 + (((lane & 7) ^ (((lane >> 4) & 1) << 2)) * 16);
    const unsigned xs0 = 32 * T * 896;
    unsigned ve = roleA ? vx : vw, vo = roleA ? vx1 : vw;
    unsigned s0 = roleA ? xs0 : w4 * 8 * N3 * 2, ds = roleA ? 7168 : 32 * N3 * 2, l0 = lds0 + (roleA ? LDS_X + w4 * 4096 : LDS_W + w4 * 1024), dl = roleA ? 1024 : 4096;
    asm volatile("" : "+v"(ve), "+v"(vo), "+s"(s0), "+s"(ds), "+s"(l0), "+s"(dl));
    SB();
    asm volatile("" : "+s"(x), "+s"(w), "+s"(wo), "+s"(ws), "+s"(out));
    const u32x4 rx = mk_rsrc(x, SEQ * DM * 2), rwt = mk_rsrc(w, h < 14 ? DM * N3 * 2 : 0);
    {
        const u32x4 r0 = roleA ? rx : rwt;
#pragma unroll
        for (int k = 0; k < 4; ++k) dma16(r0, (k & 1) ? vo : ve, s0 + k * ds, l0 + k * dl);
    }
    SB();
    const __amdgpu_buffer_rsrc_t rws = __builtin_amdgcn_make_buffer_rsrc(ws, 0, WS_END, 0x00020000);
    const int blk = (lane >> 4) & 1, q4 = (lane & 15) >> 2, p4 = lane & 3, h8 = hh ? 8 : 0;
    const unsigned xa0 = LDS_X + w4 * 4096 + c * 128 + (((hh ? 1 : 0) ^ ((c >> 1) & 7)) * 16);

    if (!roleA) {
        asm volatile("; role B" ::: "memory");
        const u32x4 rw = mk_rsrc(w, DM * N3 * 2);
#define LDW(p) do { dma16(rwt, vw, (8 * (p) + w4) * 8 * N3 * 2, lds0 + LDS_W + (8 * (p) + w4) * 1024); dma16(rwt, vw, (8 * (p) + 4 + w4) * 8 * N3 * 2, lds0 + LDS_W + (8 * (p) + 4 + w4) * 1024); } while (0)
        LDW(2);
        const unsigned vq = ((lane >> 2) * N3 + ((7 * h) >> 1) * 8 + (lane & 3) * 8) * 2;
        bf16x8 xf[28];
#define RDX(p) do { _Pragma("unroll") for (int s = 0; s < 4; ++s) xf[4 * (p) + s] = *(LAS const bf16x8*)(lds + ((p) % 6) * XSLOT + (xa0 ^ (32 * s))); } while (0)
#define LDQ(p) dma16(rw, vq, ((p) * 64 + 16 * w4) * N3 * 2, lds0 + LDS_WQ + (4 * (p) + w4) * 1024)
        WAITV(4); BAR(); LDW(3); LDW(4); RDX(0);
        WAITV(6); BAR(); LDW(5); LDW(6); RDX(1);
        WAITV(8); BAR(); RDX(2);
        WAITV(6); BAR(); LDQ(0); LDQ(1); LDQ(2); RDX(3);
        WAITV(7); BAR(); LDQ(3); LDQ(4); RDX(4);
        WAITV(7); BAR(); LDQ(5); LDQ(6); RDX(5);
        WAITV(7); BAR(); RDX(6);
        WAITV(0); sync4(misc + 3, lane);
        {
            LAS const char* qb = lds + LDS_WQ + (h8 + q4) * 64 + 32 * blk + 8 * p4;
            f32x16 aq = {};
            bf16x8 wq[28];
#pragma unroll
            for (int s = 0; s < 6; ++s) wq[s] = tr_frag(qb + 1024 * s, qb + 1024 * s + 256);
            SB();
#pragma unroll
            for (int s = 0; s < 28; ++s) { aq = MFMA32(wq[s], xf[s], aq); SB(); if (s + 6 < 28) wq[s + 6] = tr_frag(qb + 1024 * (s + 6), qb + 1024 * (s + 6) + 256); SB(); }
#pragma unroll
            for (int i = 0; i < 16; ++i) aq[i] *= QSCALE;
#pragma unroll
            for (int i = 0; i < 4; ++i) { aq[12 + i] = (hh && !(h & 1)) ? 0.f : aq[12 + i]; aq[i] = (!hh && (h & 1)) ? 0.f : aq[i]; }
            *(LAS u32x4*)(lds + LDS_Q + w4 * 2048 + lane * 16) = piece(pack_lo(aq));
            *(LAS u32x4*)(lds + LDS_Q + w4 * 2048 + 1024 + lane * 16) = piece(pack_hi(aq));
            float qn = 0.f;
#pragma unroll
            for (int i = 0; i < 16; ++i) qn = fmaf(aq[i], aq[i], qn);
            qn += swap32(qn, hh);
            lds_max(lds0 + LDS_MISC + 8, pos_bits(qn));
        }
        sync4(misc + 7, lane);
        if (h >= 14) __builtin_amdgcn_endpgm();
        const int c3 = h >= 7 ? h - 7 : h, kh = w4 >> 1, T3 = 4 * r + (h >= 7 ? 2 : 0) + (w4 & 1);
        unsigned vwo = (lane >> 3) * (DM * 2) + c3 * 128 + (((lane & 7) ^ (((lane >> 4) & 1) << 2)) * 16);
        const int sw = (q4 >> 1) & 1;
        unsigned b1 = LDS_WO + (kh * 8 * DH + (hh ? 4 : 0) + q4) * 128 + 64 * (sw ^ kh) + 32 * blk + 8 * p4;
        unsigned b3 = b1 + (hh ? 20 : 24) * 128;
        unsigned oin = WS_O + (T3 * 16 + 8 * kh) * 2048 + lane * 16;
        unsigned fpo = WS_OFLAG + (T3 * 16 + 8 * kh + (lane & 7)) * 8;
        LAS char* exs = lds + LDS_XY + ((w4 & 1) * 2 + (kh ^ 1)) * 4096 + lane * 16;
        LAS const char* exr = lds + LDS_XY + ((w4 & 1) * 2 + kh) * 4096 + lane * 16;
        GAS __bf16* orow = (GAS __bf16*)out + (size_t)(32 * T3 + c) * DM + c3 * 64 + kh * 32 + h8;
        PIN(vwo); PIN(orow); PIN(fpo); PIN(oin); PIN(b1); PIN(b3); PIN(exs); PIN(exr);
        SB();
        wait4(misc + 8);
        __builtin_amdgcn_s_setprio(BPRIO);
        {
            const u32x4 rwo = mk_rsrc(wo, DM * DM * 2);
#pragma unroll
            for (int i = 0; i < 14; ++i) dma16(rwo, vwo, (w4 + 4 * i) * 8 * DM * 2, lds0 + LDS_WO + (w4 + 4 * i) * 1024);
        }
        const u64 tag = (u64)(unsigned)__builtin_amdgcn_readfirstlane(misc[5]) | ((u64)(unsigned)__builtin_amdgcn_readfirstlane(misc[6]) << 32);
        __builtin_amdgcn_s_setprio(0);
        if (bid == 0 && w4 == 0) {
            GAS u64* sp = (GAS u64*)(ws + WS_FLAG + (((lane >> 3) & 3) * 256 + 14) * 32 + (lane & 7) * 8);
            for (unsigned spins = 0;; ++spins) { if (__all((__hip_atomic_load(sp, RLX_AGENT) >> 16) == tag) || spins > SPIN_CAP) break; __builtin_amdgcn_s_sleep(8); }
        }
        f32x16 pa[2] = {};
        bf16x8 wf[8][2][2];
        WAITV(0); BAR();
        {
#pragma unroll
            for (int i = 0; i < 8; ++i)
#pragma unroll
                for (int a = 0; a < 2; ++a) {
                    LAS const char *p1 = lds + (b1 ^ (64 * a)), *p3 = lds + (b3 ^ (64 * a));
                    wf[i][0][a] = tr_frag(p1 + i * 3584, p1 + i * 3584 + 1024); wf[i][1][a] = tr_frag(p1 + i * 3584 + 2048, p3 + i * 3584);
                    PIN(wf[i][0][a]); PIN(wf[i][1][a]); }
            u64 fv, fa, fb;
            WAIT_FLAGS(fv, fa, fb, fpo, tag, "0xff", "0");
            for (unsigned spins = 0;; ++spins) {
                MEMFENCE();
                u32x4 of[16];
#define LDO(j) of[j] = __builtin_amdgcn_raw_buffer_load_b128(rws, oin + ((j) & 1) * 1024, ((j) >> 1) * 2048, SC1)
#pragma unroll
                for (int j = 0; j < OAH; ++j) { LDO(j); SB(); }
                asm volatile("s_waitcnt vmcnt(%c2)" : "+v"(fa), "+v"(fb) : "i"(OAH) : "memory");
                unsigned mx = 0;
                pa[0] = (f32x16){}; pa[1] = (f32x16){};
#pragma unroll
                for (int j = 0; j < 16; ++j) {
                    pa[1] = MFMA32(wf[j >> 1][j & 1][1], BF(of[j]), pa[1]); SB();
                    if (j + OAH < 16) { LDO(j + OAH); SB(); }
                    mx = max(max(mx, of[j][0]), of[j][1]); mx = max(max(mx, of[j][2]), of[j][3]); PIN(mx); SB();
                    pa[0] = MFMA32(wf[j >> 1][j & 1][0], BF(of[j]), pa[0]); SB();
                }
                if (!__any(mx == SENT) || spins > SPIN_CAP) break;
            }
        }
        {
#pragma unroll
            for (int g = 0; g < 4; ++g) *(LAS f32x4*)(exs + g * 1024) = (f32x4){pa[1][4 * g], pa[1][4 * g + 1], pa[1][4 * g + 2], pa[1][4 * g + 3]};
            BAR();
#pragma unroll
            for (int g = 0; g < 4; ++g) { const f32x4 v = *(LAS const f32x4*)(exr + g * 1024);
#pragma unroll
                for (int i = 0; i < 4; ++i) pa[0][4 * g + i] += v[i]; }
            *(GAS u32x4*)(orow) = piece(pack_lo(pa[0]));
            *(GAS u32x4*)(orow + 16) = piece(pack_hi(pa[0]));
        }
        if (bid == 0 && threadIdx.x == 256) __hip_atomic_store((GAS u64*)(ws + WS_EPOCH), tag, RLX_AGENT);
        __builtin_amdgcn_endpgm();
    } else {
#define LDX(p) do { const unsigned sl = lds0 + LDS_X + ((p) % 6) * XSLOT + w4 * 4096; \
            dma16(rx, vx, xs0 + (p) * 128, sl); dma16(rx, vx1, xs0 + (p) * 128 + 8 * 896, sl + 1024); dma16(rx, vx, xs0 + (p) * 128 + 16 * 896, sl + 2048); dma16(rx, vx1, xs0 + (p) * 128 + 24 * 896, sl + 3072); } while (0)
        LDX(1); LDX(2);
        SB();
        const u64 epoch = __hip_atomic_load((GAS u64*)(ws + WS_EPOCH), RLX_AGENT);
        if (threadIdx.x < 12) misc[threadIdx.x] = 0u;
        unsigned ooff = WS_O + (T * 16 + h) * 2048 + lane * 16;
        PIN(ooff);
#define AUX() do { \
        __builtin_amdgcn_raw_buffer_store_b128((u32x4){SENT, SENT, SENT, SENT}, rws, ooff, 0, SC1); \
        __builtin_amdgcn_raw_buffer_store_b128((u32x4){SENT, SENT, SENT, SENT}, rws, ooff + 1024, 0, SC1); } while (0)
        const bool isk = h < 7, isv = h >= 7 && h < 14;
        const int ct = isk ? h : h - 7;
        unsigned dst0, dst1, dstp;
        { const int c0 = 64 * ct + c, h0 = (c0 * 293) >> 13, c1 = c0 + 32, h1 = (c1 * 293) >> 13;
          dst0 = WS_KV + T * TREC + (isk ? ct * 4096 + lane * 16 : TR_V + h0 * 2048 + (c0 - DH * h0 + (hh ? 32 : 0)) * 16);
          dst1 = WS_KV + T * TREC + TR_V + h1 * 2048 + (c1 - DH * h1 + (hh ? 32 : 0)) * 16;
          const int hp = (64 * ct) / DH + (lane >> 4);
          dstp = (DH * hp + DH - 1 < 64 * ct + 64) ? WS_KV + T * TREC + TR_V + hp * 2048 + ((lane >> 2) & 3) * 512 + (DH + (lane & 3)) * 16 : OOB;
          PIN(dst0); PIN(dst1); PIN(dstp); }
        const int sw = (q4 >> 1) & 1;
        const unsigned wa0 = LDS_W + (h8 + q4) * 128 + 64 * sw + 32 * blk + 8 * p4, wa1 = wa0 ^ 64;
        f32x16 a0 = {}, a1 = {};
        bf16x8 xf_[2][4], w0_[2][4], w1_[2][4];
#define XW(p) ((p) == 0 ? 9 : (p) == 1 ? 13 : (p) == 2 ? 19 : 4 * (6 - (p)) + 2)
#define P1M(acc, wv, p, s, KT) do { if (KT) acc = MFMA32(wv[(p) & 1][s], xf_[(p) & 1][s], acc); else acc = MFMA32(xf_[(p) & 1][s], wv[(p) & 1][s], acc); } while (0)
#define P1X(p, s) (xf_[(p) & 1][s] = *(LAS const bf16x8*)(lds + ((p) % 6) * XSLOT + (xa0 ^ (32 * (s)))))
#define P1W(wv, wa, p, s) (wv[(p) & 1][s] = tr_frag(lds + wa + 8192 * (p) + 2048 * (s), lds + wa + 8192 * (p) + 2048 * (s) + 512))
#define P1MM(acc, wv, p, s, KT) do { if ((p) > 0) { P1M(acc, wv, (p) - 1, s, KT); PIN(acc); } SB(); } while (0)
#define P1STEP(p, KT) do { WAITV(XW(p)); BAR(); SB(); if (KT < 2) { \
            P1MM(a0, w0_, p, 0, KT); P1X(p, 0); P1W(w0_, wa0, p, 0); SB(); P1MM(a1, w1_, p, 0, KT); P1W(w1_, wa1, p, 0); P1X(p, 1); SB(); \
            P1MM(a0, w0_, p, 1, KT); P1W(w0_, wa0, p, 1); P1W(w1_, wa1, p, 1); SB(); P1MM(a1, w1_, p, 1, KT); P1X(p, 2); P1W(w0_, wa0, p, 2); SB(); \
            P1MM(a0, w0_, p, 2, KT); P1W(w1_, wa1, p, 2); P1X(p, 3); SB(); P1MM(a1, w1_, p, 2, KT); P1W(w0_, wa0, p, 3); P1W(w1_, wa1, p, 3); SB(); \
            P1MM(a0, w0_, p, 3, KT); } if ((p) <= 1) { LDX(2 * (p) + 3); LDX(2 * (p) + 4); } if ((p) == 1) AUX(); SB(); if (KT < 2) P1MM(a1, w1_, p, 3, KT); } while (0)
#define P1L(acc, wv, s, KT) do { P1M(acc, wv, 6, s, KT); PIN(acc); SB(); } while (0)
#define P1LAST(KT, F, d0, d1) do { P1L(a0, w0_, 0, KT); P1L(a0, w0_, 1, KT); P1L(a0, w0_, 2, KT); P1L(a0, w0_, 3, KT); P1L(a1, w1_, 0, KT); P1L(a1, w1_, 1, KT); \
            k0 = F(pack_lo(a0)); __builtin_amdgcn_raw_buffer_store_b128(k0, rws, d0, 0, SC1); SB(); P1L(a1, w1_, 2, KT); \
            k1 = F(pack_hi(a0)); __builtin_amdgcn_raw_buffer_store_b128(k1, rws, (d0) + 1024, 0, SC1); SB(); P1L(a1, w1_, 3, KT); \
            stat = sumsq(k0) + sumsq(k1); PIN(stat); SB(); \
            k2 = F(pack_lo(a1)); __builtin_amdgcn_raw_buffer_store_b128(k2, rws, d1, 0, SC1); k3 = F(pack_hi(a1)); __builtin_amdgcn_raw_buffer_store_b128(k3, rws, (d1) + 1024, 0, SC1); SB(); \
            stat += sumsq(k2) + sumsq(k3); } while (0)
#define P1ALL(KT) do { P1STEP(0, KT); P1STEP(1, KT); P1STEP(2, KT); P1STEP(3, KT); P1STEP(4, KT); P1STEP(5, KT); P1STEP(6, KT); } while (0)
        float stat = 0.f;
        u32x4 k0, k1, k2, k3;
        if (isk) {
            P1ALL(1); P1LAST(1, piece, dst0, dst0 + 2048);
            stat += swap32(stat, hh);
        } else if (isv) {
            const unsigned one = (lane & 3) == 0 ? 0x3F803F80u : 0u;
            P1ALL(0);
            __builtin_amdgcn_raw_buffer_store_b128((u32x4){one, one, one, one}, rws, dstp, 0, SC1);
            P1LAST(0, , dst0, dst1);
        } else {
            P1ALL(2);
        }
        lds_max(lds0 + LDS_MISC + 4, pos_bits(stat));
        GAS u64* fo = (GAS u64*)(ws + WS_FLAG + (((r >> 2) * 16 + (lane & 15)) * 16 + h) * 32 + (r & 3) * 8);
        PIN(fo);
        asm volatile("s_waitcnt vmcnt(0) lgkmcnt(0)" ::: "memory");
        const u64 tag = (((u64)__builtin_amdgcn_readfirstlane((unsigned)(epoch >> 32)) << 32 | (unsigned)__builtin_amdgcn_readfirstlane((unsigned)epoch)) + 1ull) & 0xFFFFFFFFFFFFull;
        {
            if (threadIdx.x == 0) { misc[5] = (unsigned)tag; misc[6] = (unsigned)(tag >> 32); }
            unsigned arr = 0;
            if (lane == 0) arr = __hip_atomic_fetch_add(misc, 1u, RLX_WG);
            MEMFENCE();
            const unsigned sb = (__hip_atomic_load(misc + 1, RLX_WG) + 0xFFFFu) >> 16;
            if (__builtin_amdgcn_readfirstlane(arr) == 3u && lane < 16) (void)__hip_atomic_exchange(fo, tag << 16 | sb, RLX_AGENT);
        }
        const int pc0 = (7 * h) >> 1, ka = pc0 >> 3, kb = (pc0 + 3) >> 3, va = (DH * h) >> 6, vb = (DH * h + DH - 1) >> 6;
        const int l4 = (lane >> 2) & 3;
        const unsigned fpo = WS_FLAG + ((w4 * 16 + r) * 16 + (l4 == 0 ? ka : l4 == 1 ? kb : l4 == 2 ? 7 + va : 7 + vb)) * 32 + (lane & 3) * 8;
        u64 fv, fa, fb;
        SB();
        f32x16 o[4] = {};
        PIN(o[0]); PIN(o[1]); PIN(o[2]); PIN(o[3]);
        LAS char* cmb[3];
#pragma unroll
        for (int t = 0; t < 3; ++t) cmb[t] = lds + cmb_slot(((w4 + 1 + t) & 3) * 3 + 2 - t) + lane * 16;
#define SENDQ(t, g) (*(LAS f32x4*)(cmb[t] + (g) * 1024) = (f32x4){o[t][4 * (g)], o[t][4 * (g) + 1], o[t][4 * (g) + 2], o[t][4 * (g) + 3]})
#define SEND(t, mv) do { o[t][13] = hh ? (mv) : o[t][13]; SENDQ(t, 0); SENDQ(t, 1); SENDQ(t, 2); SENDQ(t, 3); } while (0)
        char *kbase = ws + WS_KV + 16 * w4 * TREC + pc0 * 512, *vbase = ws + WS_KV + 16 * w4 * TREC + TR_V + h * 2048;
        asm volatile("" : "+s"(kbase), "+s"(vbase));
        WAIT_FLAGS(fv, fa, fb, fpo, tag, "0xffff", "16");
        MEMFENCE();
        const __amdgpu_buffer_rsrc_t rk = __builtin_amdgcn_make_buffer_rsrc(kbase, 16, 16 * TREC, 1 << 23);
        const __amdgpu_buffer_rsrc_t rv = __builtin_amdgcn_make_buffer_rsrc(vbase, 16, 16 * TREC, 1 << 23);
#define LDF(rs_, f_, so_) __builtin_amdgcn_raw_buffer_load_b128(rs_, (f_) * 1024, so_, SC1)
        u32x4 kf[4][2], vf[4][2];
        kf[0][0] = LDF(rk, 0, 0); SB(); kf[0][1] = LDF(rk, 1, 0); SB(); vf[0][0] = LDF(rv, 0, 0); SB(); vf[0][1] = LDF(rv, 1, 0); SB(); kf[1][0] = LDF(rk, 0, TREC); SB(); kf[1][1] = LDF(rk, 1, TREC); SB();
        vf[3][0] = (u32x4){0u, 0u, 0u, 0u}; vf[3][1] = vf[3][0];
        bf16x8 qf[4][2];
        float qn;
        for (unsigned n = 0;; ++n) {
            MEMFENCE();
            const unsigned c0 = __hip_atomic_load(misc, RLX_WG), c7 = __hip_atomic_load(misc + 7, RLX_WG);
            MEMFENCE();
            qn = __uint_as_float(__hip_atomic_load(misc + 2, RLX_WG));
#pragma unroll
            for (int t = 0; t < 4; ++t) { const int tg = (w4 + 1 + t) & 3; qf[t][0] = *(LAS const bf16x8*)(lds + LDS_Q + tg * 2048 + lane * 16); qf[t][1] = *(LAS const bf16x8*)(lds + LDS_Q + tg * 2048 + 1024 + lane * 16); }
            if (__builtin_amdgcn_readfirstlane(min(c0, c7)) >= 4u || n > (SPIN_CAP << 6)) break;
        }
        asm volatile("s_waitcnt vmcnt(6)" : "+v"(fa), "+v"(fb) :: "memory");
        int fasti;
        { const unsigned s16 = (unsigned)fv << 16, nb = (unsigned)__builtin_amdgcn_update_dpp(0, (int)s16, 0x104  , 0x1, 0x1, false);
          const float lhs = (__uint_as_float(s16) + (ka != kb ? __uint_as_float(nb) : 0.f)) * (lane < 4 ? qn * 1.05f : 1.f), lim = lane < 4 ? SAFE_T2 : lane < 8 ? INFINITY : SAFE_V2;
          fasti = ((unsigned)__ballot(!(lhs <= lim)) & 0xFFFFu) == 0u ? 1 : 0; }
        const bool fast = fasti != 0;
        float m[4] = {0.f, 0.f, 0.f, 0.f};
        if (__builtin_expect(fast, 1)) {
            f32x16 sa, sb = {};
            u32x4 plo = {}, phi = {};
            const f32x16 zero = {};
            sa = MFMA32(BF(kf[0][0]), qf[0][0], zero); sa = MFMA32(BF(kf[0][1]), qf[0][1], sa);
            SB();
#define EX(S, i) S[i] = __builtin_amdgcn_exp2f(S[i])
#define STEP(SC, SN, B, t, X1, X2, X3, X4) do { \
            constexpr int tn = ((t) + 1) & 3, bn = (t) == 3 ? ((B) + 1) & 3 : (B), tp = ((t) + 3) & 3, bp = (t) == 0 ? ((B) + 3) & 3 : (B); \
            o[tp] = MFMA32(BF(vf[bp][0]), BF(plo), o[tp]); SB(); X1; SB(); \
            phi[2] = cvt2(SN[12], SN[13]); phi[3] = cvt2(SN[14], SN[15]); PIN(phi); SB(); EX(SC, 0); EX(SC, 1); EX(SC, 2); EX(SC, 3); PIN(SC); SB(); \
            SN = MFMA32(BF(kf[bn][0]), qf[tn][0], zero); SB(); X2; SB(); \
            plo[0] = cvt2(SC[0], SC[1]); plo[1] = cvt2(SC[2], SC[3]); PIN(plo); SB(); EX(SC, 4); EX(SC, 5); EX(SC, 6); EX(SC, 7); PIN(SC); SB(); \
            SN = MFMA32(BF(kf[bn][1]), qf[tn][1], SN); SB(); X3; SB(); \
            plo[2] = cvt2(SC[4], SC[5]); plo[3] = cvt2(SC[6], SC[7]); PIN(plo); SB(); EX(SC, 8); EX(SC, 9); EX(SC, 10); EX(SC, 11); PIN(SC); SB(); \
            o[tp] = MFMA32(BF(vf[bp][1]), BF(phi), o[tp]); SB(); X4; SB(); \
            phi[0] = cvt2(SC[8], SC[9]); phi[1] = cvt2(SC[10], SC[11]); PIN(phi); SB(); EX(SC, 12); EX(SC, 13); EX(SC, 14); EX(SC, 15); PIN(SC); SB(); \
            } while (0)
#define NOP (void)0
#define PIN4(a, b, c_, d) asm volatile("" : "+v"(a), "+v"(b), "+v"(c_), "+v"(d))
#define PINS(x) asm volatile("" : "+s"(x))
#define BLOCK(B) \
            STEP(sa, sb, B, 0, vf[((B) + 3) & 3][0] = LDF(rv, 0, so), PIN4(vf[B][0], vf[B][1], kf[((B) + 1) & 3][0], kf[((B) + 1) & 3][1]), NOP, vf[((B) + 3) & 3][1] = LDF(rv, 1, so)); \
            STEP(sb, sa, B, 1, do { so = min(so + TREC, 15 * TREC); PINS(so); } while (0), NOP, NOP, NOP); \
            STEP(sa, sb, B, 2, NOP, kf[B][0] = LDF(rk, 0, so), kf[B][1] = LDF(rk, 1, so), NOP); \
            STEP(sb, sa, B, 3, NOP, NOP, NOP, NOP)
            STEP(sa, sb, 0, 0, vf[1][0] = LDF(rv, 0, TREC), vf[1][1] = LDF(rv, 1, TREC), kf[2][0] = LDF(rk, 0, 2 * TREC), kf[2][1] = LDF(rk, 1, 2 * TREC));
            STEP(sb, sa, 0, 1, vf[2][0] = LDF(rv, 0, 2 * TREC), vf[2][1] = LDF(rv, 1, 2 * TREC), kf[3][0] = LDF(rk, 0, 3 * TREC), kf[3][1] = LDF(rk, 1, 3 * TREC));
            STEP(sa, sb, 0, 2, vf[3][0] = LDF(rv, 0, 3 * TREC), vf[3][1] = LDF(rv, 1, 3 * TREC), kf[0][0] = LDF(rk, 0, 4 * TREC), kf[0][1] = LDF(rk, 1, 4 * TREC));
            STEP(sb, sa, 0, 3, do { if (lane == 0) __hip_atomic_fetch_add(misc + 8, 1u, RLX_WG); } while (0), NOP, NOP, NOP);
            unsigned so = 4 * TREC;
            for (int it = 0; it < 3; ++it) { BLOCK(1); BLOCK(2); BLOCK(3); BLOCK(0); }
            STEP(sa, sb, 1, 0, NOP, PIN4(vf[1][0], vf[1][1], kf[2][0], kf[2][1]), NOP, NOP); STEP(sb, sa, 1, 1, NOP, NOP, NOP, NOP); STEP(sa, sb, 1, 2, NOP, NOP, NOP, NOP); STEP(sb, sa, 1, 3, NOP, NOP, NOP, NOP);
            STEP(sa, sb, 2, 0, NOP, PIN4(vf[2][0], vf[2][1], kf[3][0], kf[3][1]), NOP, NOP); STEP(sb, sa, 2, 1, NOP, NOP, NOP, NOP); STEP(sa, sb, 2, 2, NOP, NOP, NOP, NOP); STEP(sb, sa, 2, 3, NOP, NOP, NOP, NOP);
            STEP(sa, sb, 3, 0, NOP, PIN4(vf[3][0], vf[3][1], kf[3][0], kf[3][1]), NOP, NOP); STEP(sb, sa, 3, 1, NOP, NOP, NOP, NOP);
            STEP(sa, sb, 3, 2, NOP, SENDQ(0, 0), SENDQ(0, 1), SENDQ(0, 2));
            STEP(sb, sa, 3, 3, SENDQ(0, 3), SENDQ(1, 0), SENDQ(1, 1), SENDQ(1, 2));
            o[3] = MFMA32(BF(vf[3][0]), BF(plo), o[3]); SB(); SENDQ(1, 3); SB();
            phi[2] = cvt2(sb[12], sb[13]); phi[3] = cvt2(sb[14], sb[15]); SB();
            SENDQ(2, 0); SB();
            o[3] = MFMA32(BF(vf[3][1]), BF(phi), o[3]); SB();
            SENDQ(2, 1); SENDQ(2, 2); SENDQ(2, 3);
        }
        PIN(fasti);
        if (__builtin_expect(__builtin_amdgcn_readfirstlane(fasti) == 0, 0)) {
            if (lane == 0) __hip_atomic_fetch_add(misc + 8, 1u, RLX_WG);
#pragma unroll
            for (int t = 0; t < 4; ++t) m[t] = -INFINITY;
            for (int j = 0; j < 16; ++j) {
                kf[0][0] = LDF(rk, 0, j * TREC); kf[0][1] = LDF(rk, 1, j * TREC); vf[0][0] = LDF(rv, 0, j * TREC); vf[0][1] = LDF(rv, 1, j * TREC);
#pragma unroll
                for (int t = 0; t < 4; ++t) {
                    SB();
                    f32x16 s = {};
                    s = MFMA32(BF(kf[0][0]), qf[t][0], s);
                    s = MFMA32(BF(kf[0][1]), qf[t][1], s);
                    float bm = s[0];
#pragma unroll
                    for (int i = 1; i < 16; ++i) bm = fmaxf(bm, s[i]);
                    bm = fmaxf(bm, swap32(bm, hh));
                    const float mn = fmaxf(m[t], bm), al = __builtin_amdgcn_exp2f(m[t] - mn);
                    m[t] = mn;
                    o[t] *= al; PIN(o[t]); SB();
#pragma unroll
                    for (int i = 0; i < 16; ++i) s[i] = __builtin_amdgcn_exp2f(s[i] - mn);
                    PIN(s); SB();
                    o[t] = MFMA32(BF(vf[0][0]), BF(pack_lo(s)), o[t]);
                    o[t] = MFMA32(BF(vf[0][1]), BF(pack_hi(s)), o[t]);
                    PIN(o[t]);
                }
            }
            SEND(0, m[0]); SEND(1, m[1]); SEND(2, m[2]);
            o[3][13] = hh ? m[3] : o[3][13];
        }
        f32x16 acc = o[3];
        BAR();
        f32x16 p[3];
#pragma unroll
        for (int d = 0; d < 3; ++d)
#pragma unroll
            for (int g = 0; g < 4; ++g) { const f32x4 v = *(LAS const f32x4*)(lds + cmb_slot(w4 * 3 + d) + g * 1024 + lane * 16);
#pragma unroll
                for (int i = 0; i < 4; ++i) p[d][4 * g + i] = v[i]; }
        const bool plain = __all(!hh || (acc[13] == 0.f && p[0][13] == 0.f && p[1][13] == 0.f && p[2][13] == 0.f));
        if (__builtin_expect(plain, 1)) {
            acc = (acc + p[0]) + (p[1] + p[2]);
        } else {
            float mm[4] = {acc[13], p[0][13], p[1][13], p[2][13]};
#pragma unroll
            for (int d = 0; d < 4; ++d) { const float s = swap32(mm[d], hh); mm[d] = hh ? mm[d] : s; }
            const float M = fmaxf(fmaxf(mm[0], mm[1]), fmaxf(mm[2], mm[3]));
            acc *= __builtin_amdgcn_exp2f(mm[0] - M);
#pragma unroll
            for (int d = 0; d < 3; ++d) acc += p[d] * __builtin_amdgcn_exp2f(mm[d + 1] - M);
        }
        const float lsw = swap32(acc[12], hh);
        const float inv = __builtin_amdgcn_rcpf(hh ? acc[12] : lsw);
        acc *= inv;
        u32x4 lo = pack_lo(acc), hi = pack_hi(acc);
        if (hh) { hi[2] = 0u; hi[3] = 0u; }
#pragma unroll
        for (int i = 0; i < 4; ++i) { lo[i] = min(lo[i], SENT - 1u); hi[i] = min(hi[i], SENT - 1u); }
        __builtin_amdgcn_raw_buffer_store_b128(lo, rws, ooff, 0, SC1);
        __builtin_amdgcn_raw_buffer_store_b128(hi, rws, ooff + 1024, 0, SC1);
        MEMFENCE();
        if (lane == 0) (void)__hip_atomic_exchange((GAS u64*)(ws + WS_OFLAG + (T * 16 + h) * 8), tag, RLX_AGENT);
        __builtin_amdgcn_endpgm();
    }
}

extern "C" void kernel_launch(void* const* d_in, const int* in_sizes, int n_in, void* d_out, int out_size, void* d_ws, size_t ws_size, hipStream_t stream) {
    (void)hipFuncSetAttribute((const void*)k_mha, hipFuncAttributeMaxDynamicSharedMemorySize, LDS_BYTES);
    k_mha<<<256, 512, LDS_BYTES, stream>>>((const __bf16*)d_in[0], (const __bf16*)d_in[1], (const __bf16*)d_in[2], (char*)d_ws, (__bf16*)d_out);
}
```

```cpp
#include <hip/hip_runtime.h>
#include <hip/hip_bf16.h>
#include <stdint.h>

typedef __bf16 bf16x8 __attribute__((ext_vector_type(8)));
typedef __bf16 bf16x2 __attribute__((ext_vector_type(2)));
typedef float f32x16 __attribute__((ext_vector_type(16)));
typedef float f32x4 __attribute__((ext_vector_type(4)));
typedef float f32x2 __attribute__((ext_vector_type(2)));
typedef unsigned u32x4 __attribute__((ext_vector_type(4)));
typedef short s16x4 __attribute__((ext_vector_type(4)));
typedef unsigned long long u64;
#define LAS __attribute__((address_space(3)))
#define GAS __attribute__((address_space(1)))
#define DI __device__ __forceinline__
#define MFMA32(a, b, c) __builtin_amdgcn_mfma_f32_32x32x16_bf16((a), (b), (c), 0, 0, 0)
#define SB() __builtin_amdgcn_sched_barrier(0)
#define PIN(x) asm volatile("" : "+v"(x))
#define MEMFENCE() asm volatile("" ::: "memory")
#define RLX_AGENT __ATOMIC_RELAXED, __HIP_MEMORY_SCOPE_AGENT
#define RLX_WG __ATOMIC_RELAXED, __HIP_MEMORY_SCOPE_WORKGROUP
#define SC1 16
#define BF(x) __builtin_bit_cast(bf16x8, x)
#define U4(x) __builtin_bit_cast(u32x4, x)

constexpr int SEQ = 2048, DM = 448, DH = 28, N3 = 1344;
constexpr float QSCALE = 0.18898223650461362f * 1.4426950408889634f;
constexpr unsigned TREC = 61440, TR_V = 28672;
constexpr unsigned WS_KV = 4096, WS_O = 4u << 20, WS_FLAG = 6u << 20, WS_OFLAG = WS_FLAG + 32768, WS_EPOCH = WS_OFLAG + 8192, WS_END = WS_EPOCH + 256;
constexpr int LDS_W = 0, LDS_X = 57344, XSLOT = 16384, LDS_WQ = LDS_X + XSLOT, LDS_WO = LDS_X, LDS_Q = 40960, LDS_MISC = 157696, LDS_XY = LDS_WO + 57344, LDS_BYTES = LDS_MISC + 64 + 256;
DI int cmb_slot(int k) { return 4096 * (k < 10 ? k : k + 2); }
constexpr float SAFE_T2 = 90.f * 90.f;
constexpr float SAFE_V2 = 1.0e12f;
constexpr int CODE_BYTES = 29696;
constexpr unsigned SPIN_CAP = 1u << 18;
constexpr unsigned SENT = 0xFFFFFFFFu, OOB = 0xFFFFFF00u;

DI bf16x8 tr_frag(LAS const char* p0, LAS const char* p1) {
    s16x4 a = __builtin_bit_cast(s16x4, __builtin_amdgcn_ds_read_tr16_b64_v4i16((LAS s16x4*)p0));
    s16x4 b = __builtin_bit_cast(s16x4, __builtin_amdgcn_ds_read_tr16_b64_v4i16((LAS s16x4*)p1));
    return __builtin_bit_cast(bf16x8, __builtin_shufflevector(a, b, 0, 1, 2, 3, 4, 5, 6, 7));
}
DI u32x4 pack_lo(const f32x16& c) { return U4(__builtin_convertvector(__builtin_shufflevector(c, c, 0, 1, 2, 3, 4, 5, 6, 7), bf16x8)); }
DI u32x4 pack_hi(const f32x16& c) { return U4(__builtin_convertvector(__builtin_shufflevector(c, c, 8, 9, 10, 11, 12, 13, 14, 15), bf16x8)); }
DI unsigned cvt2(float a, float b) { return __builtin_bit_cast(unsigned, __builtin_convertvector((f32x2){a, b}, bf16x2)); }
DI float swap32(float v, bool hi) {
    auto r = __builtin_amdgcn_permlane32_swap(__float_as_uint(v), __float_as_uint(v), false, false);
    return hi ? __uint_as_float(r[0]) : __uint_as_float(r[1]);
}
DI u32x4 piece(u32x4 v) {
    auto a = __builtin_amdgcn_permlane32_swap(v[0], v[2], false, false), b = __builtin_amdgcn_permlane32_swap(v[1], v[3], false, false);
    return (u32x4){a[0], b[0], a[1], b[1]};
}
DI float sumsq(u32x4 v) {
    float s = 0.f;
#pragma unroll
    for (int i = 0; i < 4; ++i) { const unsigned u = v[i]; const bf16x2 p = __builtin_bit_cast(bf16x2, u); s = __builtin_amdgcn_fdot2_f32_bf16(p, p, s, false); }
    return s;
}
#define WAIT_FLAGS(res, ta, tb, off, tag_, MASK, SH) do { u64 t_; unsigned n_; \
    asm volatile("s_mov_b64 exec, " MASK "\n\ts_mov_b32 %4, %8\n\t" \
                 "v_mov_b64 %1, 0\n\tglobal_atomic_add_x2 %1, %5, %1, %6 sc0\n\ts_sleep 6\n" \
                 "1:\n\tv_mov_b64 %2, 0\n\tglobal_atomic_add_x2 %2, %5, %2, %6 sc0\n\ts_waitcnt vmcnt(1)\n\t" \
                 "v_lshrrev_b64 %3, " SH ", %1\n\tv_mov_b64 %0, %1\n\tv_cmp_ne_u64 vcc, %7, %3\n\ts_cbranch_vccz 2f\n\t" \
                 "v_mov_b64 %1, 0\n\tglobal_atomic_add_x2 %1, %5, %1, %6 sc0\n\ts_waitcnt vmcnt(1)\n\t" \
                 "v_lshrrev_b64 %3, " SH ", %2\n\tv_mov_b64 %0, %2\n\tv_cmp_ne_u64 vcc, %7, %3\n\ts_cbranch_vccz 2f\n\t" \
                 "s_sub_u32 %4, %4, 1\n\ts_cbranch_scc0 1b\n" \
                 "2:\n\ts_mov_b64 exec, -1" \
                 : "=&v"(res), "=&v"(ta), "=&v"(tb), "=&v"(t_), "=&s"(n_) : "v"(off), "s"(ws), "s"(tag_), "i"(SPIN_CAP) : "memory", "vcc", "scc"); } while (0)
DI unsigned pos_bits(float v) { return (v == v) ? __float_as_uint(v) : 0x7F800000u; }
DI void dma16(u32x4 rsrc, unsigned voff, unsigned soff, unsigned lds_dst) {
    unsigned keep;
    asm volatile("s_mov_b32 %0, m0\n\ts_mov_b32 m0, %4\n\ts_nop 0\n\tbuffer_load_dwordx4 %1, %2, %3 offen lds\n\ts_mov_b32 m0, %0"
                 : "=&s"(keep) : "v"(voff), "s"(rsrc), "s"(soff), "s"(lds_dst) : "memory");
}
DI u32x4 mk_rsrc(const void* p, unsigned bytes) { const u64 a = (u64)p; return (u32x4){(unsigned)a, (unsigned)(a >> 32) & 0xffffu, bytes, 0x00020000u}; }
#define WAITV(n) asm volatile("s_waitcnt vmcnt(%c0)" :: "i"(n) : "memory")
#define BAR() asm volatile("s_waitcnt lgkmcnt(0)\n\ts_barrier" ::: "memory")
DI void lds_max(unsigned addr, unsigned v) { asm volatile("ds_max_u32 %0, %1" :: "v"(addr), "v"(v) : "memory"); }
DI void wait4(LAS unsigned* cnt) {
    for (unsigned n = 0; __builtin_amdgcn_readfirstlane(__hip_atomic_load(cnt, RLX_WG)) < 4u && n < (SPIN_CAP << 6); ++n) __builtin_amdgcn_s_sleep(1);
    MEMFENCE();
}
DI void sync4(LAS unsigned* cnt, int lane) {
    asm volatile("s_waitcnt lgkmcnt(0)" ::: "memory");
    if (lane == 0) __hip_atomic_fetch_add(cnt, 1u, RLX_WG);
    wait4(cnt);
}

__global__ __launch_bounds__(512, 2) void k_mha(const __bf16* x, const __bf16* w, const __bf16* wo, char* ws, __bf16* out) {
    extern __shared__ __attribute__((aligned(16))) char lds_[];
    { u64 pc_; unsigned keep; const unsigned po = min((blockIdx.x >> 3) * 1024u + (threadIdx.x >> 5) * 64u, (unsigned)CODE_BYTES);
      asm volatile("s_mov_b32 %1, m0\n\ts_mov_b32 m0, %3\n\ts_getpc_b64 %0\n\tglobal_load_lds_dword %2, %0\n\ts_mov_b32 m0, %1"
                   : "=&s"(pc_), "=&s"(keep) : "v"(po), "s"((unsigned)(uintptr_t)(LAS char*)lds_ + LDS_MISC + 64) : "memory"); }
    LAS char* lds = (LAS char*)lds_;
    LAS unsigned* misc = (LAS unsigned*)(lds + LDS_MISC);
    const unsigned lds0 = (unsigned)(uintptr_t)lds;
    const int bid = blockIdx.x;
    const int h = (bid & 1) * 8 + ((bid >> 3) & 7), r = ((bid >> 1) & 3) * 4 + (bid >> 6);
    const int lane = threadIdx.x & 63, wid = __builtin_amdgcn_readfirstlane(threadIdx.x >> 6), w4 = wid & 3;
    const int c = lane & 31; const bool hh = lane >= 32;
    const int T = 4 * r + w4;
    const bool roleA = wid < 4;
    const unsigned vx = (lane >> 3) * 896 + (((lane & 7) ^ (lane >> 4)) * 16), vx1 = vx ^ 64;
    const unsigned vw = (lane >> 3) * (N3 * 2) + 896 + (h < 14 ? h : 0) * 128 + (((lane & 7) ^ (((lane >> 4) & 1) << 2)) * 16);
    const unsigned xs0 = 32 * T * 896;
    unsigned ve = roleA ? vx : vw, vo = roleA ? vx1 : vw;
    unsigned s0 = roleA ? xs0 : w4 * 8 * N3 * 2, ds = roleA ? 7168 : 32 * N3 * 2, l0 = lds0 + (roleA ? LDS_X + w4 * 4096 : LDS_W + w4 * 1024), dl = roleA ? 1024 : 4096;
    asm volatile("" : "+v"(ve), "+v"(vo), "+s"(s0), "+s"(ds), "+s"(l0), "+s"(dl));
    SB();
    asm volatile("" : "+s"(x), "+s"(w), "+s"(wo), "+s"(ws), "+s"(out));
    const u32x4 rx = mk_rsrc(x, SEQ * DM * 2), rwt = mk_rsrc(w, h < 14 ? DM * N3 * 2 : 0);
    {
        const u32x4 r0 = roleA ? rx : rwt;
#pragma unroll
        for (int k = 0; k < 4; ++k) dma16(r0, (k & 1) ? vo : ve, s0 + k * ds, l0 + k * dl);
    }
    SB();
    const __amdgpu_buffer_rsrc_t rws = __builtin_amdgcn_make_buffer_rsrc(ws, 0, WS_END, 0x00020000);
    const int blk = (lane >> 4) & 1, q4 = (lane & 15) >> 2, p4 = lane & 3, h8 = hh ? 8 : 0;
    const unsigned xa0 = LDS_X + w4 * 4096 + c * 128 + (((hh ? 1 : 0) ^ ((c >> 1) & 7)) * 16);

    if (!roleA) {
        asm volatile("; role B" ::: "memory");
        const u32x4 rw = mk_rsrc(w, DM * N3 * 2);
#define LDW(p) do { dma16(rwt, vw, (8 * (p) + w4) * 8 * N3 * 2, lds0 + LDS_W + (8 * (p) + w4) * 1024); dma16(rwt, vw, (8 * (p) + 4 + w4) * 8 * N3 * 2, lds0 + LDS_W + (8 * (p) + 4 + w4) * 1024); } while (0)
        LDW(2);
        const unsigned vq = ((lane >> 2) * N3 + ((7 * h) >> 1) * 8 + (lane & 3) * 8) * 2;
        bf16x8 xf[28];
#define RDX(p) do { _Pragma("unroll") for (int s = 0; s < 4; ++s) xf[4 * (p) + s] = *(LAS const bf16x8*)(lds + ((p) % 6) * XSLOT + (xa0 ^ (32 * s))); } while (0)
#define LDQ(p) dma16(rw, vq, ((p) * 64 + 16 * w4) * N3 * 2, lds0 + LDS_WQ + (4 * (p) + w4) * 1024)
        WAITV(4); BAR(); LDW(3); LDW(4); RDX(0);
        WAITV(6); BAR(); LDW(5); LDW(6); RDX(1);
        WAITV(8); BAR(); RDX(2);
        WAITV(6); BAR(); LDQ(0); LDQ(1); LDQ(2); RDX(3);
        WAITV(7); BAR(); LDQ(3); LDQ(4); RDX(4);
        WAITV(7); BAR(); LDQ(5); LDQ(6); RDX(5);
        WAITV(7); BAR(); RDX(6);
        WAITV(0); sync4(misc + 3, lane);
        {
            LAS const char* qb = lds + LDS_WQ + (h8 + q4) * 64 + 32 * blk + 8 * p4;
            f32x16 aq = {};
            bf16x8 wq[28];
#pragma unroll
            for (int s = 0; s < 6; ++s) wq[s] = tr_frag(qb + 1024 * s, qb + 1024 * s + 256);
            SB();
#pragma unroll
            for (int s = 0; s < 28; ++s) { aq = MFMA32(wq[s], xf[s], aq); SB(); if (s + 6 < 28) wq[s + 6] = tr_frag(qb + 1024 * (s + 6), qb + 1024 * (s + 6) + 256); SB(); }
#pragma unroll
            for (int i = 0; i < 16; ++i) aq[i] *= QSCALE;
#pragma unroll
            for (int i = 0; i < 4; ++i) { aq[12 + i] = (hh && !(h & 1)) ? 0.f : aq[12 + i]; aq[i] = (!hh && (h & 1)) ? 0.f : aq[i]; }
            *(LAS u32x4*)(lds + LDS_Q + w4 * 2048 + lane * 16) = piece(pack_lo(aq));
            *(LAS u32x4*)(lds + LDS_Q + w4 * 2048 + 1024 + lane * 16) = piece(pack_hi(aq));
            float qn = 0.f;
#pragma unroll
            for (int i = 0; i < 16; ++i) qn = fmaf(aq[i], aq[i], qn);
            qn += swap32(qn, hh);
            lds_max(lds0 + LDS_MISC + 8, pos_bits(qn));
        }
        sync4(misc + 7, lane);
        if (h >= 14) __builtin_amdgcn_endpgm();
        const int c3 = h >= 7 ? h - 7 : h, kh = w4 >> 1, T3 = 4 * r + (h >= 7 ? 2 : 0) + (w4 & 1);
        wait4(misc + 8);
        {
            const u32x4 rwo = mk_rsrc(wo, DM * DM * 2);
            const unsigned vo = (lane >> 3) * (DM * 2) + c3 * 128 + (((lane & 7) ^ (((lane >> 4) & 1) << 2)) * 16);
#pragma unroll
            for (int i = 0; i < 14; ++i) dma16(rwo, vo, (w4 + 4 * i) * 8 * DM * 2, lds0 + LDS_WO + (w4 + 4 * i) * 1024);
        }
        const u64 tag = (u64)misc[5] | ((u64)misc[6] << 32);
        const int sw = (q4 >> 1) & 1;
        unsigned b1 = LDS_WO + (kh * 8 * DH + (hh ? 4 : 0) + q4) * 128 + 64 * (sw ^ kh) + 32 * blk + 8 * p4;
        unsigned b3 = b1 + (hh ? 20 : 24) * 128;
        unsigned oin = WS_O + (T3 * 16 + 8 * kh) * 2048 + lane * 16;
        unsigned fpo = WS_OFLAG + (T3 * 16 + 8 * kh + (lane & 7)) * 8;
        LAS char* exs = lds + LDS_XY + ((w4 & 1) * 2 + (kh ^ 1)) * 4096 + lane * 16;
        LAS const char* exr = lds + LDS_XY + ((w4 & 1) * 2 + kh) * 4096 + lane * 16;
        GAS __bf16* orow = (GAS __bf16*)out + (size_t)(32 * T3 + c) * DM + c3 * 64 + kh * 32 + h8;
        PIN(orow); PIN(fpo); PIN(oin); PIN(b1); PIN(b3); PIN(exs); PIN(exr);
        if (bid == 0 && w4 == 0) {
            GAS u64* sp = (GAS u64*)(ws + WS_FLAG + (((lane >> 3) & 3) * 256 + 14) * 32 + (lane & 7) * 8);
            for (unsigned spins = 0;; ++spins) { if (__all((__hip_atomic_load(sp, RLX_AGENT) >> 16) == tag) || spins > SPIN_CAP) break; __builtin_amdgcn_s_sleep(8); }
        }
        f32x16 pa[2] = {};
        bf16x8 wf[8][2][2];
        WAITV(0); BAR();
        {
#pragma unroll
            for (int i = 0; i < 8; ++i)
#pragma unroll
                for (int a = 0; a < 2; ++a) {
                    LAS const char *p1 = lds + (b1 ^ (64 * a)), *p3 = lds + (b3 ^ (64 * a));
                    wf[i][0][a] = tr_frag(p1 + i * 3584, p1 + i * 3584 + 1024); wf[i][1][a] = tr_frag(p1 + i * 3584 + 2048, p3 + i * 3584);
                    PIN(wf[i][0][a]); PIN(wf[i][1][a]); }
            u64 fv, fa, fb;
            WAIT_FLAGS(fv, fa, fb, fpo, tag, "0xff", "0");
            for (unsigned spins = 0;; ++spins) {
                MEMFENCE();
                u32x4 of[8][2];
#pragma unroll
                for (int i = 0; i < 8; ++i) { of[i][0] = __builtin_amdgcn_raw_buffer_load_b128(rws, oin, i * 2048, SC1); SB(); of[i][1] = __builtin_amdgcn_raw_buffer_load_b128(rws, oin + 1024, i * 2048, SC1); SB(); }
                asm volatile("s_waitcnt vmcnt(16)" : "+v"(fa), "+v"(fb) :: "memory");
                unsigned mx = 0;
                pa[0] = (f32x16){}; pa[1] = (f32x16){};
#pragma unroll
                for (int i = 0; i < 8; ++i)
#pragma unroll
                    for (int k = 0; k < 2; ++k) {
                        pa[1] = MFMA32(wf[i][k][1], BF(of[i][k]), pa[1]); SB();
                        mx = max(max(mx, of[i][k][0]), of[i][k][1]); mx = max(max(mx, of[i][k][2]), of[i][k][3]); PIN(mx); SB();
                        pa[0] = MFMA32(wf[i][k][0], BF(of[i][k]), pa[0]); SB();
                    }
                if (!__any(mx == SENT) || spins > SPIN_CAP) break;
            }
        }
        {
#pragma unroll
            for (int g = 0; g < 4; ++g) *(LAS f32x4*)(exs + g * 1024) = (f32x4){pa[1][4 * g], pa[1][4 * g + 1], pa[1][4 * g + 2], pa[1][4 * g + 3]};
            BAR();
#pragma unroll
            for (int g = 0; g < 4; ++g) { const f32x4 v = *(LAS const f32x4*)(exr + g * 1024);
#pragma unroll
                for (int i = 0; i < 4; ++i) pa[0][4 * g + i] += v[i]; }
            *(GAS u32x4*)(orow) = piece(pack_lo(pa[0]));
            *(GAS u32x4*)(orow + 16) = piece(pack_hi(pa[0]));
        }
        if (bid == 0 && threadIdx.x == 256) __hip_atomic_store((GAS u64*)(ws + WS_EPOCH), tag, RLX_AGENT);
        __builtin_amdgcn_endpgm();
    } else {
#define LDX(p) do { const unsigned sl = lds0 + LDS_X + ((p) % 6) * XSLOT + w4 * 4096; \
            dma16(rx, vx, xs0 + (p) * 128, sl); dma16(rx, vx1, xs0 + (p) * 128 + 8 * 896, sl + 1024); dma16(rx, vx, xs0 + (p) * 128 + 16 * 896, sl + 2048); dma16(rx, vx1, xs0 + (p) * 128 + 24 * 896, sl + 3072); } while (0)
        LDX(1); LDX(2);
        SB();
        const u64 epoch = __hip_atomic_load((GAS u64*)(ws + WS_EPOCH), RLX_AGENT);
        if (threadIdx.x < 12) misc[threadIdx.x] = 0u;
        unsigned ooff = WS_O + (T * 16 + h) * 2048 + lane * 16;
        PIN(ooff);
#define AUX() do { \
        __builtin_amdgcn_raw_buffer_store_b128((u32x4){SENT, SENT, SENT, SENT}, rws, ooff, 0, SC1); \
        __builtin_amdgcn_raw_buffer_store_b128((u32x4){SENT, SENT, SENT, SENT}, rws, ooff + 1024, 0, SC1); } while (0)
        const bool isk = h < 7, isv = h >= 7 && h < 14;
        const int ct = isk ? h : h - 7;
        unsigned dst0, dst1, dstp;
        { const int c0 = 64 * ct + c, h0 = (c0 * 293) >> 13, c1 = c0 + 32, h1 = (c1 * 293) >> 13;
          dst0 = WS_KV + T * TREC + (isk ? ct * 4096 + lane * 16 : TR_V + h0 * 2048 + (c0 - DH * h0 + (hh ? 32 : 0)) * 16);
          dst1 = WS_KV + T * TREC + TR_V + h1 * 2048 + (c1 - DH * h1 + (hh ? 32 : 0)) * 16;
          const int hp = (64 * ct) / DH + (lane >> 4);
          dstp = (DH * hp + DH - 1 < 64 * ct + 64) ? WS_KV + T * TREC + TR_V + hp * 2048 + ((lane >> 2) & 3) * 512 + (DH + (lane & 3)) * 16 : OOB;
          PIN(dst0); PIN(dst1); PIN(dstp); }
        const int sw = (q4 >> 1) & 1;
        const unsigned wa0 = LDS_W + (h8 + q4) * 128 + 64 * sw + 32 * blk + 8 * p4, wa1 = wa0 ^ 64;
        f32x16 a0 = {}, a1 = {};
        bf16x8 xf_[2][4], w0_[2][4], w1_[2][4];
#define XW(p) ((p) == 0 ? 9 : (p) == 1 ? 13 : (p) == 2 ? 19 : 4 * (6 - (p)) + 2)
#define P1M(acc, wv, p, s, KT) do { if (KT) acc = MFMA32(wv[(p) & 1][s], xf_[(p) & 1][s], acc); else acc = MFMA32(xf_[(p) & 1][s], wv[(p) & 1][s], acc); } while (0)
#define P1X(p, s) (xf_[(p) & 1][s] = *(LAS const bf16x8*)(lds + ((p) % 6) * XSLOT + (xa0 ^ (32 * (s)))))
#define P1W(wv, wa, p, s) (wv[(p) & 1][s] = tr_frag(lds + wa + 8192 * (p) + 2048 * (s), lds + wa + 8192 * (p) + 2048 * (s) + 512))
#define P1MM(acc, wv, p, s, KT) do { if ((p) > 0) { P1M(acc, wv, (p) - 1, s, KT); PIN(acc); } SB(); } while (0)
#define P1STEP(p, KT) do { WAITV(XW(p)); BAR(); SB(); if (KT < 2) { \
            P1MM(a0, w0_, p, 0, KT); P1X(p, 0); P1W(w0_, wa0, p, 0); SB(); P1MM(a1, w1_, p, 0, KT); P1W(w1_, wa1, p, 0); P1X(p, 1); SB(); \
            P1MM(a0, w0_, p, 1, KT); P1W(w0_, wa0, p, 1); P1W(w1_, wa1, p, 1); SB(); P1MM(a1, w1_, p, 1, KT); P1X(p, 2); P1W(w0_, wa0, p, 2); SB(); \
            P1MM(a0, w0_, p, 2, KT); P1W(w1_, wa1, p, 2); P1X(p, 3); SB(); P1MM(a1, w1_, p, 2, KT); P1W(w0_, wa0, p, 3); P1W(w1_, wa1, p, 3); SB(); \
            P1MM(a0, w0_, p, 3, KT); } if ((p) <= 1) { LDX(2 * (p) + 3); LDX(2 * (p) + 4); } if ((p) == 1) AUX(); SB(); if (KT < 2) P1MM(a1, w1_, p, 3, KT); } while (0)
#define P1L(acc, wv, s, KT) do { P1M(acc, wv, 6, s, KT); PIN(acc); SB(); } while (0)
#define P1LAST(KT, F, d0, d1) do { P1L(a0, w0_, 0, KT); P1L(a0, w0_, 1, KT); P1L(a0, w0_, 2, KT); P1L(a0, w0_, 3, KT); P1L(a1, w1_, 0, KT); P1L(a1, w1_, 1, KT); \
            k0 = F(pack_lo(a0)); __builtin_amdgcn_raw_buffer_store_b128(k0, rws, d0, 0, SC1); SB(); P1L(a1, w1_, 2, KT); \
            k1 = F(pack_hi(a0)); __builtin_amdgcn_raw_buffer_store_b128(k1, rws, (d0) + 1024, 0, SC1); SB(); P1L(a1, w1_, 3, KT); \
            stat = sumsq(k0) + sumsq(k1); PIN(stat); SB(); \
            k2 = F(pack_lo(a1)); __builtin_amdgcn_raw_buffer_store_b128(k2, rws, d1, 0, SC1); k3 = F(pack_hi(a1)); __builtin_amdgcn_raw_buffer_store_b128(k3, rws, (d1) + 1024, 0, SC1); SB(); \
            stat += sumsq(k2) + sumsq(k3); } while (0)
#define P1ALL(KT) do { P1STEP(0, KT); P1STEP(1, KT); P1STEP(2, KT); P1STEP(3, KT); P1STEP(4, KT); P1STEP(5, KT); P1STEP(6, KT); } while (0)
        float stat = 0.f;
        u32x4 k0, k1, k2, k3;
        if (isk) {
            P1ALL(1); P1LAST(1, piece, dst0, dst0 + 2048);
            stat += swap32(stat, hh);
        } else if (isv) {
            const unsigned one = (lane & 3) == 0 ? 0x3F803F80u : 0u;
            P1ALL(0);
            __builtin_amdgcn_raw_buffer_store_b128((u32x4){one, one, one, one}, rws, dstp, 0, SC1);
            P1LAST(0, , dst0, dst1);
        } else {
            P1ALL(2);
        }
        lds_max(lds0 + LDS_MISC + 4, pos_bits(stat));
        GAS u64* fo = (GAS u64*)(ws + WS_FLAG + (((r >> 2) * 16 + (lane & 15)) * 16 + h) * 32 + (r & 3) * 8);
        PIN(fo);
        asm volatile("s_waitcnt vmcnt(0) lgkmcnt(0)" ::: "memory");
        const u64 tag = (((u64)__builtin_amdgcn_readfirstlane((unsigned)(epoch >> 32)) << 32 | (unsigned)__builtin_amdgcn_readfirstlane((unsigned)epoch)) + 1ull) & 0xFFFFFFFFFFFFull;
        {
            if (threadIdx.x == 0) { misc[5] = (unsigned)tag; misc[6] = (unsigned)(tag >> 32); }
            unsigned arr = 0;
            if (lane == 0) arr = __hip_atomic_fetch_add(misc, 1u, RLX_WG);
            MEMFENCE();
            const unsigned sb = (__hip_atomic_load(misc + 1, RLX_WG) + 0xFFFFu) >> 16;
            if (__builtin_amdgcn_readfirstlane(arr) == 3u && lane < 16) (void)__hip_atomic_exchange(fo, tag << 16 | sb, RLX_AGENT);
        }
        const int pc0 = (7 * h) >> 1, ka = pc0 >> 3, kb = (pc0 + 3) >> 3, va = (DH * h) >> 6, vb = (DH * h + DH - 1) >> 6;
        const int l4 = (lane >> 2) & 3;
        const unsigned fpo = WS_FLAG + ((w4 * 16 + r) * 16 + (l4 == 0 ? ka : l4 == 1 ? kb : l4 == 2 ? 7 + va : 7 + vb)) * 32 + (lane & 3) * 8;
        u64 fv, fa, fb;
        SB();
        f32x16 o[4] = {};
        PIN(o[0]); PIN(o[1]); PIN(o[2]); PIN(o[3]);
        wait4(misc); wait4(misc + 7);
        bf16x8 qf[4][2];
#pragma unroll
        for (int t = 0; t < 4; ++t) { const int tg = (w4 + 1 + t) & 3; qf[t][0] = *(LAS const bf16x8*)(lds + LDS_Q + tg * 2048 + lane * 16); qf[t][1] = *(LAS const bf16x8*)(lds + LDS_Q + tg * 2048 + 1024 + lane * 16); }
        const float qn = __uint_as_float(misc[2]);
        LAS char* cmb[3];
#pragma unroll
        for (int t = 0; t < 3; ++t) cmb[t] = lds + cmb_slot(((w4 + 1 + t) & 3) * 3 + 2 - t) + lane * 16;
#define SENDQ(t, g) (*(LAS f32x4*)(cmb[t] + (g) * 1024) = (f32x4){o[t][4 * (g)], o[t][4 * (g) + 1], o[t][4 * (g) + 2], o[t][4 * (g) + 3]})
#define SEND(t, mv) do { o[t][13] = hh ? (mv) : o[t][13]; SENDQ(t, 0); SENDQ(t, 1); SENDQ(t, 2); SENDQ(t, 3); } while (0)
        WAIT_FLAGS(fv, fa, fb, fpo, tag, "0xffff", "16");
        MEMFENCE();
        const __amdgpu_buffer_rsrc_t rk = __builtin_amdgcn_make_buffer_rsrc(ws + WS_KV + 16 * w4 * TREC + pc0 * 512, 16, 16 * TREC, 1 << 23);
        const __amdgpu_buffer_rsrc_t rv = __builtin_amdgcn_make_buffer_rsrc(ws + WS_KV + 16 * w4 * TREC + TR_V + h * 2048, 16, 16 * TREC, 1 << 23);
#define LDF(rs_, f_, so_) __builtin_amdgcn_raw_buffer_load_b128(rs_, (f_) * 1024, so_, SC1)
        u32x4 kf[4][2], vf[4][2];
#pragma unroll
        for (int b = 0; b < 4; ++b) { kf[b][0] = LDF(rk, 0, b * TREC); SB(); kf[b][1] = LDF(rk, 1, b * TREC); SB();
            if (b < 3) { vf[b][0] = LDF(rv, 0, b * TREC); SB(); vf[b][1] = LDF(rv, 1, b * TREC); SB(); } }
        vf[3][0] = (u32x4){0u, 0u, 0u, 0u}; vf[3][1] = vf[3][0];
        if (lane == 0) __hip_atomic_fetch_add(misc + 8, 1u, RLX_WG);
        SB();
        asm volatile("s_waitcnt vmcnt(14)" : "+v"(fa), "+v"(fb) :: "memory");
        unsigned sa_ = 0, sb_ = 0, sv_ = 0;
        { const unsigned s16 = (unsigned)fv << 16;
#pragma unroll
          for (int i = 0; i < 4; ++i) { sa_ = max(sa_, (unsigned)__builtin_amdgcn_readlane(s16, i)); sb_ = max(sb_, (unsigned)__builtin_amdgcn_readlane(s16, 4 + i));
              sv_ = max(sv_, max((unsigned)__builtin_amdgcn_readlane(s16, 8 + i), (unsigned)__builtin_amdgcn_readlane(s16, 12 + i))); } }
        const float kn = __uint_as_float(sa_) + (ka != kb ? __uint_as_float(sb_) : 0.f);
        int fasti = (kn * qn * 1.05f <= SAFE_T2 && __uint_as_float(sv_) <= SAFE_V2) ? 1 : 0;
        const bool fast = __builtin_amdgcn_readfirstlane(fasti) != 0;
        float m[4] = {0.f, 0.f, 0.f, 0.f};
        if (__builtin_expect(fast, 1)) {
            f32x16 sa, sb = {};
            u32x4 plo = {}, phi = {};
            const f32x16 zero = {};
            sa = MFMA32(BF(kf[0][0]), qf[0][0], zero); sa = MFMA32(BF(kf[0][1]), qf[0][1], sa);
            SB();
#define EX(S, i) S[i] = __builtin_amdgcn_exp2f(S[i])
#define STEP(SC, SN, B, t, X1, X2, X3, X4) do { \
            constexpr int tn = ((t) + 1) & 3, bn = (t) == 3 ? ((B) + 1) & 3 : (B), tp = ((t) + 3) & 3, bp = (t) == 0 ? ((B) + 3) & 3 : (B); \
            o[tp] = MFMA32(BF(vf[bp][0]), BF(plo), o[tp]); SB(); X1; SB(); \
            phi[2] = cvt2(SN[12], SN[13]); phi[3] = cvt2(SN[14], SN[15]); PIN(phi); SB(); EX(SC, 0); EX(SC, 1); EX(SC, 2); EX(SC, 3); PIN(SC); SB(); \
            SN = MFMA32(BF(kf[bn][0]), qf[tn][0], zero); SB(); X2; SB(); \
            plo[0] = cvt2(SC[0], SC[1]); plo[1] = cvt2(SC[2], SC[3]); PIN(plo); SB(); EX(SC, 4); EX(SC, 5); EX(SC, 6); EX(SC, 7); PIN(SC); SB(); \
            SN = MFMA32(BF(kf[bn][1]), qf[tn][1], SN); SB(); X3; SB(); \
            plo[2] = cvt2(SC[4], SC[5]); plo[3] = cvt2(SC[6], SC[7]); PIN(plo); SB(); EX(SC, 8); EX(SC, 9); EX(SC, 10); EX(SC, 11); PIN(SC); SB(); \
            o[tp] = MFMA32(BF(vf[bp][1]), BF(phi), o[tp]); SB(); X4; SB(); \
            phi[0] = cvt2(SC[8], SC[9]); phi[1] = cvt2(SC[10], SC[11]); PIN(phi); SB(); EX(SC, 12); EX(SC, 13); EX(SC, 14); EX(SC, 15); PIN(SC); SB(); \
            } while (0)
#define NOP (void)0
#define PIN4(a, b, c_, d) asm volatile("" : "+v"(a), "+v"(b), "+v"(c_), "+v"(d))
#define PINS(x) asm volatile("" : "+s"(x))
#define BLOCK(B) \
            STEP(sa, sb, B, 0, vf[((B) + 3) & 3][0] = LDF(rv, 0, so), PIN4(vf[B][0], vf[B][1], kf[((B) + 1) & 3][0], kf[((B) + 1) & 3][1]), NOP, vf[((B) + 3) & 3][1] = LDF(rv, 1, so)); \
            STEP(sb, sa, B, 1, do { so += TREC; PINS(so); } while (0), NOP, NOP, NOP); \
            STEP(sa, sb, B, 2, NOP, kf[B][0] = LDF(rk, 0, so), kf[B][1] = LDF(rk, 1, so), NOP); \
            STEP(sb, sa, B, 3, NOP, NOP, NOP, NOP)
            unsigned so = 3 * TREC;
            for (int it = 0; it < 3; ++it) { BLOCK(0); BLOCK(1); BLOCK(2); BLOCK(3); }
            STEP(sa, sb, 0, 0, vf[3][0] = LDF(rv, 0, so), PIN4(vf[0][0], vf[0][1], kf[1][0], kf[1][1]), NOP, vf[3][1] = LDF(rv, 1, so));
            STEP(sb, sa, 0, 1, NOP, NOP, NOP, NOP); STEP(sa, sb, 0, 2, NOP, NOP, NOP, NOP); STEP(sb, sa, 0, 3, NOP, NOP, NOP, NOP);
            STEP(sa, sb, 1, 0, NOP, PIN4(vf[1][0], vf[1][1], kf[2][0], kf[2][1]), NOP, NOP); STEP(sb, sa, 1, 1, NOP, NOP, NOP, NOP); STEP(sa, sb, 1, 2, NOP, NOP, NOP, NOP); STEP(sb, sa, 1, 3, NOP, NOP, NOP, NOP);
            STEP(sa, sb, 2, 0, NOP, PIN4(vf[2][0], vf[2][1], kf[3][0], kf[3][1]), NOP, NOP); STEP(sb, sa, 2, 1, NOP, NOP, NOP, NOP); STEP(sa, sb, 2, 2, NOP, NOP, NOP, NOP); STEP(sb, sa, 2, 3, NOP, NOP, NOP, NOP);
            STEP(sa, sb, 3, 0, NOP, PIN4(vf[3][0], vf[3][1], kf[3][0], kf[3][1]), NOP, NOP); STEP(sb, sa, 3, 1, NOP, NOP, NOP, NOP);
            STEP(sa, sb, 3, 2, NOP, SENDQ(0, 0), SENDQ(0, 1), SENDQ(0, 2));
            STEP(sb, sa, 3, 3, SENDQ(0, 3), SENDQ(1, 0), SENDQ(1, 1), SENDQ(1, 2));
            o[3] = MFMA32(BF(vf[3][0]), BF(plo), o[3]); SB(); SENDQ(1, 3); SB();
            phi[2] = cvt2(sb[12], sb[13]); phi[3] = cvt2(sb[14], sb[15]); SB();
            SENDQ(2, 0); SB();
            o[3] = MFMA32(BF(vf[3][1]), BF(phi), o[3]); SB();
            SENDQ(2, 1); SENDQ(2, 2); SENDQ(2, 3);
        }
        PIN(fasti);
        if (__builtin_expect(__builtin_amdgcn_readfirstlane(fasti) == 0, 0)) {
#pragma unroll
            for (int t = 0; t < 4; ++t) m[t] = -INFINITY;
            for (int j = 0; j < 16; ++j) {
                kf[0][0] = LDF(rk, 0, j * TREC); kf[0][1] = LDF(rk, 1, j * TREC); vf[0][0] = LDF(rv, 0, j * TREC); vf[0][1] = LDF(rv, 1, j * TREC);
#pragma unroll
                for (int t = 0; t < 4; ++t) {
                    SB();
                    f32x16 s = {};
                    s = MFMA32(BF(kf[0][0]), qf[t][0], s);
                    s = MFMA32(BF(kf[0][1]), qf[t][1], s);
                    float bm = s[0];
#pragma unroll
                    for (int i = 1; i < 16; ++i) bm = fmaxf(bm, s[i]);
                    bm = fmaxf(bm, swap32(bm, hh));
                    const float mn = fmaxf(m[t], bm), al = __builtin_amdgcn_exp2f(m[t] - mn);
                    m[t] = mn;
                    o[t] *= al; PIN(o[t]); SB();
#pragma unroll
                    for (int i = 0; i < 16; ++i) s[i] = __builtin_amdgcn_exp2f(s[i] - mn);
                    PIN(s); SB();
                    o[t] = MFMA32(BF(vf[0][0]), BF(pack_lo(s)), o[t]);
                    o[t] = MFMA32(BF(vf[0][1]), BF(pack_hi(s)), o[t]);
                    PIN(o[t]);
                }
            }
            SEND(0, m[0]); SEND(1, m[1]); SEND(2, m[2]);
            o[3][13] = hh ? m[3] : o[3][13];
        }
        f32x16 acc = o[3];
        BAR();
        f32x16 p[3];
#pragma unroll
        for (int d = 0; d < 3; ++d)
#pragma unroll
            for (int g = 0; g < 4; ++g) { const f32x4 v = *(LAS const f32x4*)(lds + cmb_slot(w4 * 3 + d) + g * 1024 + lane * 16);
#pragma unroll
                for (int i = 0; i < 4; ++i) p[d][4 * g + i] = v[i]; }
        const bool plain = __all(!hh || (acc[13] == 0.f && p[0][13] == 0.f && p[1][13] == 0.f && p[2][13] == 0.f));
        if (__builtin_expect(plain, 1)) {
            acc = (acc + p[0]) + (p[1] + p[2]);
        } else {
            float mm[4] = {acc[13], p[0][13], p[1][13], p[2][13]};
#pragma unroll
            for (int d = 0; d < 4; ++d) { const float s = swap32(mm[d], hh); mm[d] = hh ? mm[d] : s; }
            const float M = fmaxf(fmaxf(mm[0], mm[1]), fmaxf(mm[2], mm[3]));
            acc *= __builtin_amdgcn_exp2f(mm[0] - M);
#pragma unroll
            for (int d = 0; d < 3; ++d) acc += p[d] * __builtin_amdgcn_exp2f(mm[d + 1] - M);
        }
        const float lsw = swap32(acc[12], hh);
        const float inv = __builtin_amdgcn_rcpf(hh ? acc[12] : lsw);
        acc *= inv;
        u32x4 lo = pack_lo(acc), hi = pack_hi(acc);
        if (hh) { hi[2] = 0u; hi[3] = 0u; }
#pragma unroll
        for (int i = 0; i < 4; ++i) { lo[i] = min(lo[i], SENT - 1u); hi[i] = min(hi[i], SENT - 1u); }
        __builtin_amdgcn_raw_buffer_store_b128(lo, rws, ooff, 0, SC1);
        __builtin_amdgcn_raw_buffer_store_b128(hi, rws, ooff + 1024, 0, SC1);
        MEMFENCE();
        if (lane == 0) (void)__hip_atomic_exchange((GAS u64*)(ws + WS_OFLAG + (T * 16 + h) * 8), tag, RLX_AGENT);
        __builtin_amdgcn_endpgm();
    }
}

extern "C" void kernel_launch(void* const* d_in, const int* in_sizes, int n_in, void* d_out, int out_size, void* d_ws, size_t ws_size, hipStream_t stream) {
    (void)hipFuncSetAttribute((const void*)k_mha, hipFuncAttributeMaxDynamicSharedMemorySize, LDS_BYTES);
    k_mha<<<256, 512, LDS_BYTES, stream>>>((const __bf16*)d_in[0], (const __bf16*)d_in[1], (const __bf16*)d_in[2], (char*)d_ws, (__bf16*)d_out);
}
```

```cpp
#include <hip/hip_runtime.h>
#include <hip/hip_bf16.h>
#include <stdint.h>

typedef __bf16 bf16x8 __attribute__((ext_vector_type(8)));
typedef __bf16 bf16x2 __attribute__((ext_vector_type(2)));
typedef float f32x16 __attribute__((ext_vector_type(16)));
typedef float f32x4 __attribute__((ext_vector_type(4)));
typedef float f32x2 __attribute__((ext_vector_type(2)));
typedef unsigned u32x4 __attribute__((ext_vector_type(4)));
typedef short s16x4 __attribute__((ext_vector_type(4)));
typedef unsigned long long u64;
#define LAS __attribute__((address_space(3)))
#define GAS __attribute__((address_space(1)))
#define DI __device__ __forceinline__
#define MFMA32(a, b, c) __builtin_amdgcn_mfma_f32_32x32x16_bf16((a), (b), (c), 0, 0, 0)
#define SB() __builtin_amdgcn_sched_barrier(0)
#define PIN(x) asm volatile("" : "+v"(x))
#define MEMFENCE() asm volatile("" ::: "memory")
#define RLX_AGENT __ATOMIC_RELAXED, __HIP_MEMORY_SCOPE_AGENT
#define RLX_WG __ATOMIC_RELAXED, __HIP_MEMORY_SCOPE_WORKGROUP
#define SC1 16
#define BF(x) __builtin_bit_cast(bf16x8, x)
#define U4(x) __builtin_bit_cast(u32x4, x)

constexpr int SEQ = 2048, DM = 448, DH = 28, N3 = 1344;
constexpr float QSCALE = 0.18898223650461362f * 1.4426950408889634f;
constexpr unsigned TREC = 61440, TR_V = 28672;
constexpr unsigned WS_KV = 4096, WS_O = 4u << 20, WS_FLAG = 6u << 20, WS_OFLAG = WS_FLAG + 32768, WS_EPOCH = WS_OFLAG + 8192, WS_END = WS_EPOCH + 256;
constexpr int LDS_W = 0, LDS_X = 57344, XSLOT = 16384, LDS_WQ = LDS_X + XSLOT, LDS_WO = LDS_X, LDS_Q = 40960, LDS_MISC = 157696, LDS_XY = LDS_WO + 57344, LDS_BYTES = LDS_MISC + 64 + 256;
DI int cmb_slot(int k) { return 4096 * (k < 10 ? k : k + 2); }
constexpr float SAFE_T2 = 90.f * 90.f;
constexpr float SAFE_V2 = 1.0e12f;
constexpr int OAH = 6;
constexpr int CODE_BYTES = 29696;
constexpr unsigned SPIN_CAP = 1u << 18;
constexpr unsigned SENT = 0xFFFFFFFFu, OOB = 0xFFFFFF00u;

DI bf16x8 tr_frag(LAS const char* p0, LAS const char* p1) {
    s16x4 a = __builtin_bit_cast(s16x4, __builtin_amdgcn_ds_read_tr16_b64_v4i16((LAS s16x4*)p0));
    s16x4 b = __builtin_bit_cast(s16x4, __builtin_amdgcn_ds_read_tr16_b64_v4i16((LAS s16x4*)p1));
    return __builtin_bit_cast(bf16x8, __builtin_shufflevector(a, b, 0, 1, 2, 3, 4, 5, 6, 7));
}
DI u32x4 pack_lo(const f32x16& c) { return U4(__builtin_convertvector(__builtin_shufflevector(c, c, 0, 1, 2, 3, 4, 5, 6, 7), bf16x8)); }
DI u32x4 pack_hi(const f32x16& c) { return U4(__builtin_convertvector(__builtin_shufflevector(c, c, 8, 9, 10, 11, 12, 13, 14, 15), bf16x8)); }
DI unsigned cvt2(float a, float b) { return __builtin_bit_cast(unsigned, __builtin_convertvector((f32x2){a, b}, bf16x2)); }
DI float swap32(float v, bool hi) {
    auto r = __builtin_amdgcn_permlane32_swap(__float_as_uint(v), __float_as_uint(v), false, false);
    return hi ? __uint_as_float(r[0]) : __uint_as_float(r[1]);
}
DI u32x4 piece(u32x4 v) {
    auto a = __builtin_amdgcn_permlane32_swap(v[0], v[2], false, false), b = __builtin_amdgcn_permlane32_swap(v[1], v[3], false, false);
    return (u32x4){a[0], b[0], a[1], b[1]};
}
DI float sumsq(u32x4 v) {
    float s = 0.f;
#pragma unroll
    for (int i = 0; i < 4; ++i) { const unsigned u = v[i]; const bf16x2 p = __builtin_bit_cast(bf16x2, u); s = __builtin_amdgcn_fdot2_f32_bf16(p, p, s, false); }
    return s;
}
#define WAIT_FLAGS(res, ta, tb, off, tag_, MASK, SH) do { u64 t_; unsigned n_; \
    asm volatile("s_mov_b64 exec, " MASK "\n\ts_mov_b32 %4, %8\n\t" \
                 "v_mov_b64 %1, 0\n\tglobal_atomic_add_x2 %1, %5, %1, %6 sc0\n\ts_sleep 6\n" \
                 "1:\n\tv_mov_b64 %2, 0\n\tglobal_atomic_add_x2 %2, %5, %2, %6 sc0\n\ts_waitcnt vmcnt(1)\n\t" \
                 "v_lshrrev_b64 %3, " SH ", %1\n\tv_mov_b64 %0, %1\n\tv_cmp_ne_u64 vcc, %7, %3\n\ts_cbranch_vccz 2f\n\t" \
                 "v_mov_b64 %1, 0\n\tglobal_atomic_add_x2 %1, %5, %1, %6 sc0\n\ts_waitcnt vmcnt(1)\n\t" \
                 "v_lshrrev_b64 %3, " SH ", %2\n\tv_mov_b64 %0, %2\n\tv_cmp_ne_u64 vcc, %7, %3\n\ts_cbranch_vccz 2f\n\t" \
                 "s_sub_u32 %4, %4, 1\n\ts_cbranch_scc0 1b\n" \
                 "2:\n\ts_mov_b64 exec, -1" \
                 : "=&v"(res), "=&v"(ta), "=&v"(tb), "=&v"(t_), "=&s"(n_) : "v"(off), "s"(ws), "s"(tag_), "i"(SPIN_CAP) : "memory", "vcc", "scc"); } while (0)
DI unsigned pos_bits(float v) { return (v == v) ? __float_as_uint(v) : 0x7F800000u; }
DI void dma16(u32x4 rsrc, unsigned voff, unsigned soff, unsigned lds_dst) {
    unsigned keep;
    asm volatile("s_mov_b32 %0, m0\n\ts_mov_b32 m0, %4\n\ts_nop 0\n\tbuffer_load_dwordx4 %1, %2, %3 offen lds\n\ts_mov_b32 m0, %0"
                 : "=&s"(keep) : "v"(voff), "s"(rsrc), "s"(soff), "s"(lds_dst) : "memory");
}
DI u32x4 mk_rsrc(const void* p, unsigned bytes) { const u64 a = (u64)p; return (u32x4){(unsigned)a, (unsigned)(a >> 32) & 0xffffu, bytes, 0x00020000u}; }
#define WAITV(n) asm volatile("s_waitcnt vmcnt(%c0)" :: "i"(n) : "memory")
#define BAR() asm volatile("s_waitcnt lgkmcnt(0)\n\ts_barrier" ::: "memory")
DI void lds_max(unsigned addr, unsigned v) { asm volatile("ds_max_u32 %0, %1" :: "v"(addr), "v"(v) : "memory"); }
DI void wait4(LAS unsigned* cnt) {
    for (unsigned n = 0; __builtin_amdgcn_readfirstlane(__hip_atomic_load(cnt, RLX_WG)) < 4u && n < (SPIN_CAP << 6); ++n) __builtin_amdgcn_s_sleep(1);
    MEMFENCE();
}
DI void sync4(LAS unsigned* cnt, int lane) {
    asm volatile("s_waitcnt lgkmcnt(0)" ::: "memory");
    if (lane == 0) __hip_atomic_fetch_add(cnt, 1u, RLX_WG);
    wait4(cnt);
}

__global__ __launch_bounds__(512, 2) void k_mha(const __bf16* x, const __bf16* w, const __bf16* wo, char* ws, __bf16* out) {
    extern __shared__ __attribute__((aligned(16))) char lds_[];
    { u64 pc_; unsigned keep; const unsigned po = min((blockIdx.x >> 3) * 1024u + (threadIdx.x >> 5) * 64u, (unsigned)CODE_BYTES);
      asm volatile("s_mov_b32 %1, m0\n\ts_mov_b32 m0, %3\n\ts_getpc_b64 %0\n\tglobal_load_lds_dword %2, %0\n\ts_mov_b32 m0, %1"
                   : "=&s"(pc_), "=&s"(keep) : "v"(po), "s"((unsigned)(uintptr_t)(LAS char*)lds_ + LDS_MISC + 64) : "memory"); }
    LAS char* lds = (LAS char*)lds_;
    LAS unsigned* misc = (LAS unsigned*)(lds + LDS_MISC);
    const unsigned lds0 = (unsigned)(uintptr_t)lds;
    const int bid = blockIdx.x;
    const int h = (bid & 1) * 8 + ((bid >> 3) & 7), r = ((bid >> 1) & 3) * 4 + (bid >> 6);
    const int lane = threadIdx.x & 63, wid = __builtin_amdgcn_readfirstlane(threadIdx.x >> 6), w4 = wid & 3;
    const int c = lane & 31; const bool hh = lane >= 32;
    const int T = 4 * r + w4;
    const bool roleA = wid < 4;
    const unsigned vx = (lane >> 3) * 896 + (((lane & 7) ^ (lane >> 4)) * 16), vx1 = vx ^ 64;
    const unsigned vw = (lane >> 3) * (N3 * 2) + 896 + (h < 14 ? h : 0) * 128 + (((lane & 7) ^ (((lane >> 4) & 1) << 2)) * 16);
    const unsigned xs0 = 32 * T * 896;
    unsigned ve = roleA ? vx : vw, vo = roleA ? vx1 : vw;
    unsigned s0 = roleA ? xs0 : w4 * 8 * N3 * 2, ds = roleA ? 7168 : 32 * N3 * 2, l0 = lds0 + (roleA ? LDS_X + w4 * 4096 : LDS_W + w4 * 1024), dl = roleA ? 1024 : 4096;
    asm volatile("" : "+v"(ve), "+v"(vo), "+s"(s0), "+s"(ds), "+s"(l0), "+s"(dl));
    SB();
    asm volatile("" : "+s"(x), "+s"(w), "+s"(wo), "+s"(ws), "+s"(out));
    const u32x4 rx = mk_rsrc(x, SEQ * DM * 2), rwt = mk_rsrc(w, h < 14 ? DM * N3 * 2 : 0);
    {
        const u32x4 r0 = roleA ? rx : rwt;
#pragma unroll
        for (int k = 0; k < 4; ++k) dma16(r0, (k & 1) ? vo : ve, s0 + k * ds, l0 + k * dl);
    }
    SB();
    const __amdgpu_buffer_rsrc_t rws = __builtin_amdgcn_make_buffer_rsrc(ws, 0, WS_END, 0x00020000);
    const int blk = (lane >> 4) & 1, q4 = (lane & 15) >> 2, p4 = lane & 3, h8 = hh ? 8 : 0;
    const unsigned xa0 = LDS_X + w4 * 4096 + c * 128 + (((hh ? 1 : 0) ^ ((c >> 1) & 7)) * 16);

    if (!roleA) {
        asm volatile("; role B" ::: "memory");
        const u32x4 rw = mk_rsrc(w, DM * N3 * 2);
#define LDW(p) do { dma16(rwt, vw, (8 * (p) + w4) * 8 * N3 * 2, lds0 + LDS_W + (8 * (p) + w4) * 1024); dma16(rwt, vw, (8 * (p) + 4 + w4) * 8 * N3 * 2, lds0 + LDS_W + (8 * (p) + 4 + w4) * 1024); } while (0)
        LDW(2);
        const unsigned vq = ((lane >> 2) * N3 + ((7 * h) >> 1) * 8 + (lane & 3) * 8) * 2;
        bf16x8 xf[28];
#define RDX(p) do { _Pragma("unroll") for (int s = 0; s < 4; ++s) xf[4 * (p) + s] = *(LAS const bf16x8*)(lds + ((p) % 6) * XSLOT + (xa0 ^ (32 * s))); } while (0)
#define LDQ(p) dma16(rw, vq, ((p) * 64 + 16 * w4) * N3 * 2, lds0 + LDS_WQ + (4 * (p) + w4) * 1024)
        WAITV(4); BAR(); LDW(3); LDW(4); RDX(0);
        WAITV(6); BAR(); LDW(5); LDW(6); RDX(1);
        WAITV(8); BAR(); RDX(2);
        WAITV(6); BAR(); LDQ(0); LDQ(1); LDQ(2); RDX(3);
        WAITV(7); BAR(); LDQ(3); LDQ(4); RDX(4);
        WAITV(7); BAR(); LDQ(5); LDQ(6); RDX(5);
        WAITV(7); BAR(); RDX(6);
        WAITV(0); sync4(misc + 3, lane);
        {
            LAS const char* qb = lds + LDS_WQ + (h8 + q4) * 64 + 32 * blk + 8 * p4;
            f32x16 aq = {};
            bf16x8 wq[28];
#pragma unroll
            for (int s = 0; s < 6; ++s) wq[s] = tr_frag(qb + 1024 * s, qb + 1024 * s + 256);
            SB();
#pragma unroll
            for (int s = 0; s < 28; ++s) { aq = MFMA32(wq[s], xf[s], aq); SB(); if (s + 6 < 28) wq[s + 6] = tr_frag(qb + 1024 * (s + 6), qb + 1024 * (s + 6) + 256); SB(); }
#pragma unroll
            for (int i = 0; i < 16; ++i) aq[i] *= QSCALE;
#pragma unroll
            for (int i = 0; i < 4; ++i) { aq[12 + i] = (hh && !(h & 1)) ? 0.f : aq[12 + i]; aq[i] = (!hh && (h & 1)) ? 0.f : aq[i]; }
            *(LAS u32x4*)(lds + LDS_Q + w4 * 2048 + lane * 16) = piece(pack_lo(aq));
            *(LAS u32x4*)(lds + LDS_Q + w4 * 2048 + 1024 + lane * 16) = piece(pack_hi(aq));
            float qn = 0.f;
#pragma unroll
            for (int i = 0; i < 16; ++i) qn = fmaf(aq[i], aq[i], qn);
            qn += swap32(qn, hh);
            lds_max(lds0 + LDS_MISC + 8, pos_bits(qn));
        }
        sync4(misc + 7, lane);
        if (h >= 14) __builtin_amdgcn_endpgm();
        const int c3 = h >= 7 ? h - 7 : h, kh = w4 >> 1, T3 = 4 * r + (h >= 7 ? 2 : 0) + (w4 & 1);
        unsigned vwo = (lane >> 3) * (DM * 2) + c3 * 128 + (((lane & 7) ^ (((lane >> 4) & 1) << 2)) * 16);
        const int sw = (q4 >> 1) & 1;
        unsigned b1 = LDS_WO + (kh * 8 * DH + (hh ? 4 : 0) + q4) * 128 + 64 * (sw ^ kh) + 32 * blk + 8 * p4;
        unsigned b3 = b1 + (hh ? 20 : 24) * 128;
        unsigned oin = WS_O + (T3 * 16 + 8 * kh) * 2048 + lane * 16;
        unsigned fpo = WS_OFLAG + (T3 * 16 + 8 * kh + (lane & 7)) * 8;
        LAS char* exs = lds + LDS_XY + ((w4 & 1) * 2 + (kh ^ 1)) * 4096 + lane * 16;
        LAS const char* exr = lds + LDS_XY + ((w4 & 1) * 2 + kh) * 4096 + lane * 16;
        GAS __bf16* orow = (GAS __bf16*)out + (size_t)(32 * T3 + c) * DM + c3 * 64 + kh * 32 + h8;
        PIN(vwo); PIN(orow); PIN(fpo); PIN(oin); PIN(b1); PIN(b3); PIN(exs); PIN(exr);
        SB();
        wait4(misc + 8);
        __builtin_amdgcn_s_setprio(2);
        {
            const u32x4 rwo = mk_rsrc(wo, DM * DM * 2);
#pragma unroll
            for (int i = 0; i < 14; ++i) dma16(rwo, vwo, (w4 + 4 * i) * 8 * DM * 2, lds0 + LDS_WO + (w4 + 4 * i) * 1024);
        }
        const u64 tag = (u64)(unsigned)__builtin_amdgcn_readfirstlane(misc[5]) | ((u64)(unsigned)__builtin_amdgcn_readfirstlane(misc[6]) << 32);
        __builtin_amdgcn_s_setprio(0);
        if (bid == 0 && w4 == 0) {
            GAS u64* sp = (GAS u64*)(ws + WS_FLAG + (((lane >> 3) & 3) * 256 + 14) * 32 + (lane & 7) * 8);
            for (unsigned spins = 0;; ++spins) { if (__all((__hip_atomic_load(sp, RLX_AGENT) >> 16) == tag) || spins > SPIN_CAP) break; __builtin_amdgcn_s_sleep(8); }
        }
        f32x16 pa[2] = {};
        bf16x8 wf[8][2][2];
        WAITV(0); BAR();
        {
#pragma unroll
            for (int i = 0; i < 8; ++i)
#pragma unroll
                for (int a = 0; a < 2; ++a) {
                    LAS const char *p1 = lds + (b1 ^ (64 * a)), *p3 = lds + (b3 ^ (64 * a));
                    wf[i][0][a] = tr_frag(p1 + i * 3584, p1 + i * 3584 + 1024); wf[i][1][a] = tr_frag(p1 + i * 3584 + 2048, p3 + i * 3584);
                    PIN(wf[i][0][a]); PIN(wf[i][1][a]); }
            u64 fv, fa, fb;
            WAIT_FLAGS(fv, fa, fb, fpo, tag, "0xff", "0");
            for (unsigned spins = 0;; ++spins) {
                MEMFENCE();
                u32x4 of[16];
#define LDO(j) of[j] = __builtin_amdgcn_raw_buffer_load_b128(rws, oin + ((j) & 1) * 1024, ((j) >> 1) * 2048, SC1)
#pragma unroll
                for (int j = 0; j < OAH; ++j) { LDO(j); SB(); }
                asm volatile("s_waitcnt vmcnt(%c2)" : "+v"(fa), "+v"(fb) : "i"(OAH) : "memory");
                unsigned mx = 0;
                pa[0] = (f32x16){}; pa[1] = (f32x16){};
#pragma unroll
                for (int j = 0; j < 16; ++j) {
                    pa[1] = MFMA32(wf[j >> 1][j & 1][1], BF(of[j]), pa[1]); SB();
                    if (j + OAH < 16) { LDO(j + OAH); SB(); }
                    mx = max(max(mx, of[j][0]), of[j][1]); mx = max(max(mx, of[j][2]), of[j][3]); PIN(mx); SB();
                    pa[0] = MFMA32(wf[j >> 1][j & 1][0], BF(of[j]), pa[0]); SB();
                }
                if (!__any(mx == SENT) || spins > SPIN_CAP) break;
            }
        }
        {
#pragma unroll
            for (int g = 0; g < 4; ++g) *(LAS f32x4*)(exs + g * 1024) = (f32x4){pa[1][4 * g], pa[1][4 * g + 1], pa[1][4 * g + 2], pa[1][4 * g + 3]};
            BAR();
#pragma unroll
            for (int g = 0; g < 4; ++g) { const f32x4 v = *(LAS const f32x4*)(exr + g * 1024);
#pragma unroll
                for (int i = 0; i < 4; ++i) pa[0][4 * g + i] += v[i]; }
            *(GAS u32x4*)(orow) = piece(pack_lo(pa[0]));
            *(GAS u32x4*)(orow + 16) = piece(pack_hi(pa[0]));
        }
        if (bid == 0 && threadIdx.x == 256) __hip_atomic_store((GAS u64*)(ws + WS_EPOCH), tag, RLX_AGENT);
        __builtin_amdgcn_endpgm();
    } else {
#define LDX(p) do { const unsigned sl = lds0 + LDS_X + ((p) % 6) * XSLOT + w4 * 4096; \
            dma16(rx, vx, xs0 + (p) * 128, sl); dma16(rx, vx1, xs0 + (p) * 128 + 8 * 896, sl + 1024); dma16(rx, vx, xs0 + (p) * 128 + 16 * 896, sl + 2048); dma16(rx, vx1, xs0 + (p) * 128 + 24 * 896, sl + 3072); } while (0)
        LDX(1); LDX(2);
        SB();
        const u64 epoch = __hip_atomic_load((GAS u64*)(ws + WS_EPOCH), RLX_AGENT);
        if (threadIdx.x < 12) misc[threadIdx.x] = 0u;
        unsigned ooff = WS_O + (T * 16 + h) * 2048 + lane * 16;
        PIN(ooff);
#define AUX() do { \
        __builtin_amdgcn_raw_buffer_store_b128((u32x4){SENT, SENT, SENT, SENT}, rws, ooff, 0, SC1); \
        __builtin_amdgcn_raw_buffer_store_b128((u32x4){SENT, SENT, SENT, SENT}, rws, ooff + 1024, 0, SC1); } while (0)
        const bool isk = h < 7, isv = h >= 7 && h < 14;
        const int ct = isk ? h : h - 7;
        unsigned dst0, dst1, dstp;
        { const int c0 = 64 * ct + c, h0 = (c0 * 293) >> 13, c1 = c0 + 32, h1 = (c1 * 293) >> 13;
          dst0 = WS_KV + T * TREC + (isk ? ct * 4096 + lane * 16 : TR_V + h0 * 2048 + (c0 - DH * h0 + (hh ? 32 : 0)) * 16);
          dst1 = WS_KV + T * TREC + TR_V + h1 * 2048 + (c1 - DH * h1 + (hh ? 32 : 0)) * 16;
          const int hp = (64 * ct) / DH + (lane >> 4);
          dstp = (DH * hp + DH - 1 < 64 * ct + 64) ? WS_KV + T * TREC + TR_V + hp * 2048 + ((lane >> 2) & 3) * 512 + (DH + (lane & 3)) * 16 : OOB;
          PIN(dst0); PIN(dst1); PIN(dstp); }
        const int sw = (q4 >> 1) & 1;
        const unsigned wa0 = LDS_W + (h8 + q4) * 128 + 64 * sw + 32 * blk + 8 * p4, wa1 = wa0 ^ 64;
        f32x16 a0 = {}, a1 = {};
        bf16x8 xf_[2][4], w0_[2][4], w1_[2][4];
#define XW(p) ((p) == 0 ? 9 : (p) == 1 ? 13 : (p) == 2 ? 19 : 4 * (6 - (p)) + 2)
#define P1M(acc, wv, p, s, KT) do { if (KT) acc = MFMA32(wv[(p) & 1][s], xf_[(p) & 1][s], acc); else acc = MFMA32(xf_[(p) & 1][s], wv[(p) & 1][s], acc); } while (0)
#define P1X(p, s) (xf_[(p) & 1][s] = *(LAS const bf16x8*)(lds + ((p) % 6) * XSLOT + (xa0 ^ (32 * (s)))))
#define P1W(wv, wa, p, s) (wv[(p) & 1][s] = tr_frag(lds + wa + 8192 * (p) + 2048 * (s), lds + wa + 8192 * (p) + 2048 * (s) + 512))
#define P1MM(acc, wv, p, s, KT) do { if ((p) > 0) { P1M(acc, wv, (p) - 1, s, KT); PIN(acc); } SB(); } while (0)
#define P1STEP(p, KT) do { WAITV(XW(p)); BAR(); SB(); if (KT < 2) { \
            P1MM(a0, w0_, p, 0, KT); P1X(p, 0); P1W(w0_, wa0, p, 0); SB(); P1MM(a1, w1_, p, 0, KT); P1W(w1_, wa1, p, 0); P1X(p, 1); SB(); \
            P1MM(a0, w0_, p, 1, KT); P1W(w0_, wa0, p, 1); P1W(w1_, wa1, p, 1); SB(); P1MM(a1, w1_, p, 1, KT); P1X(p, 2); P1W(w0_, wa0, p, 2); SB(); \
            P1MM(a0, w0_, p, 2, KT); P1W(w1_, wa1, p, 2); P1X(p, 3); SB(); P1MM(a1, w1_, p, 2, KT); P1W(w0_, wa0, p, 3); P1W(w1_, wa1, p, 3); SB(); \
            P1MM(a0, w0_, p, 3, KT); } if ((p) <= 1) { LDX(2 * (p) + 3); LDX(2 * (p) + 4); } if ((p) == 1) AUX(); SB(); if (KT < 2) P1MM(a1, w1_, p, 3, KT); } while (0)
#define P1L(acc, wv, s, KT) do { P1M(acc, wv, 6, s, KT); PIN(acc); SB(); } while (0)
#define P1LAST(KT, F, d0, d1) do { P1L(a0, w0_, 0, KT); P1L(a0, w0_, 1, KT); P1L(a0, w0_, 2, KT); P1L(a0, w0_, 3, KT); P1L(a1, w1_, 0, KT); P1L(a1, w1_, 1, KT); \
            k0 = F(pack_lo(a0)); __builtin_amdgcn_raw_buffer_store_b128(k0, rws, d0, 0, SC1); SB(); P1L(a1, w1_, 2, KT); \
            k1 = F(pack_hi(a0)); __builtin_amdgcn_raw_buffer_store_b128(k1, rws, (d0) + 1024, 0, SC1); SB(); P1L(a1, w1_, 3, KT); \
            stat = sumsq(k0) + sumsq(k1); PIN(stat); SB(); \
            k2 = F(pack_lo(a1)); __builtin_amdgcn_raw_buffer_store_b128(k2, rws, d1, 0, SC1); k3 = F(pack_hi(a1)); __builtin_amdgcn_raw_buffer_store_b128(k3, rws, (d1) + 1024, 0, SC1); SB(); \
            stat += sumsq(k2) + sumsq(k3); } while (0)
#define P1ALL(KT) do { P1STEP(0, KT); P1STEP(1, KT); P1STEP(2, KT); P1STEP(3, KT); P1STEP(4, KT); P1STEP(5, KT); P1STEP(6, KT); } while (0)
        float stat = 0.f;
        u32x4 k0, k1, k2, k3;
        if (isk) {
            P1ALL(1); P1LAST(1, piece, dst0, dst0 + 2048);
            stat += swap32(stat, hh);
        } else if (isv) {
            const unsigned one = (lane & 3) == 0 ? 0x3F803F80u : 0u;
            P1ALL(0);
            __builtin_amdgcn_raw_buffer_store_b128((u32x4){one, one, one, one}, rws, dstp, 0, SC1);
            P1LAST(0, , dst0, dst1);
        } else {
            P1ALL(2);
        }
        lds_max(lds0 + LDS_MISC + 4, pos_bits(stat));
        GAS u64* fo = (GAS u64*)(ws + WS_FLAG + (((r >> 2) * 16 + (lane & 15)) * 16 + h) * 32 + (r & 3) * 8);
        PIN(fo);
        asm volatile("s_waitcnt vmcnt(0) lgkmcnt(0)" ::: "memory");
        const u64 tag = (((u64)__builtin_amdgcn_readfirstlane((unsigned)(epoch >> 32)) << 32 | (unsigned)__builtin_amdgcn_readfirstlane((unsigned)epoch)) + 1ull) & 0xFFFFFFFFFFFFull;
        {
            if (threadIdx.x == 0) { misc[5] = (unsigned)tag; misc[6] = (unsigned)(tag >> 32); }
            unsigned arr = 0;
            if (lane == 0) arr = __hip_atomic_fetch_add(misc, 1u, RLX_WG);
            MEMFENCE();
            const unsigned sb = (__hip_atomic_load(misc + 1, RLX_WG) + 0xFFFFu) >> 16;
            if (__builtin_amdgcn_readfirstlane(arr) == 3u && lane < 16) (void)__hip_atomic_exchange(fo, tag << 16 | sb, RLX_AGENT);
        }
        const int pc0 = (7 * h) >> 1, ka = pc0 >> 3, kb = (pc0 + 3) >> 3, va = (DH * h) >> 6, vb = (DH * h + DH - 1) >> 6;
        const int l4 = (lane >> 2) & 3;
        const unsigned fpo = WS_FLAG + ((w4 * 16 + r) * 16 + (l4 == 0 ? ka : l4 == 1 ? kb : l4 == 2 ? 7 + va : 7 + vb)) * 32 + (lane & 3) * 8;
        u64 fv, fa, fb;
        SB();
        f32x16 o[4] = {};
        PIN(o[0]); PIN(o[1]); PIN(o[2]); PIN(o[3]);
        LAS char* cmb[3];
#pragma unroll
        for (int t = 0; t < 3; ++t) cmb[t] = lds + cmb_slot(((w4 + 1 + t) & 3) * 3 + 2 - t) + lane * 16;
#define SENDQ(t, g) (*(LAS f32x4*)(cmb[t] + (g) * 1024) = (f32x4){o[t][4 * (g)], o[t][4 * (g) + 1], o[t][4 * (g) + 2], o[t][4 * (g) + 3]})
#define SEND(t, mv) do { o[t][13] = hh ? (mv) : o[t][13]; SENDQ(t, 0); SENDQ(t, 1); SENDQ(t, 2); SENDQ(t, 3); } while (0)
        char *kbase = ws + WS_KV + 16 * w4 * TREC + pc0 * 512, *vbase = ws + WS_KV + 16 * w4 * TREC + TR_V + h * 2048;
        asm volatile("" : "+s"(kbase), "+s"(vbase));
        WAIT_FLAGS(fv, fa, fb, fpo, tag, "0xffff", "16");
        MEMFENCE();
        const __amdgpu_buffer_rsrc_t rk = __builtin_amdgcn_make_buffer_rsrc(kbase, 16, 16 * TREC, 1 << 23);
        const __amdgpu_buffer_rsrc_t rv = __builtin_amdgcn_make_buffer_rsrc(vbase, 16, 16 * TREC, 1 << 23);
#define LDF(rs_, f_, so_) __builtin_amdgcn_raw_buffer_load_b128(rs_, (f_) * 1024, so_, SC1)
        u32x4 kf[4][2], vf[4][2];
        kf[0][0] = LDF(rk, 0, 0); SB(); kf[0][1] = LDF(rk, 1, 0); SB(); vf[0][0] = LDF(rv, 0, 0); SB(); vf[0][1] = LDF(rv, 1, 0); SB(); kf[1][0] = LDF(rk, 0, TREC); SB(); kf[1][1] = LDF(rk, 1, TREC); SB();
        vf[3][0] = (u32x4){0u, 0u, 0u, 0u}; vf[3][1] = vf[3][0];
        bf16x8 qf[4][2];
        float qn;
        for (unsigned n = 0;; ++n) {
            MEMFENCE();
            const unsigned c0 = __hip_atomic_load(misc, RLX_WG), c7 = __hip_atomic_load(misc + 7, RLX_WG);
            MEMFENCE();
            qn = __uint_as_float(__hip_atomic_load(misc + 2, RLX_WG));
#pragma unroll
            for (int t = 0; t < 4; ++t) { const int tg = (w4 + 1 + t) & 3; qf[t][0] = *(LAS const bf16x8*)(lds + LDS_Q + tg * 2048 + lane * 16); qf[t][1] = *(LAS const bf16x8*)(lds + LDS_Q + tg * 2048 + 1024 + lane * 16); }
            if (__builtin_amdgcn_readfirstlane(min(c0, c7)) >= 4u || n > (SPIN_CAP << 6)) break;
        }
        asm volatile("s_waitcnt vmcnt(6)" : "+v"(fa), "+v"(fb) :: "memory");
        int fasti;
        { const unsigned s16 = (unsigned)fv << 16, nb = (unsigned)__builtin_amdgcn_update_dpp(0, (int)s16, 0x104  , 0x1, 0x1, false);
          const float lhs = (__uint_as_float(s16) + (ka != kb ? __uint_as_float(nb) : 0.f)) * (lane < 4 ? qn * 1.05f : 1.f), lim = lane < 4 ? SAFE_T2 : lane < 8 ? INFINITY : SAFE_V2;
          fasti = ((unsigned)__ballot(!(lhs <= lim)) & 0xFFFFu) == 0u ? 1 : 0; }
        const bool fast = fasti != 0;
        float m[4] = {0.f, 0.f, 0.f, 0.f};
        if (__builtin_expect(fast, 1)) {
            f32x16 sa, sb = {};
            u32x4 plo = {}, phi = {};
            const f32x16 zero = {};
            sa = MFMA32(BF(kf[0][0]), qf[0][0], zero); sa = MFMA32(BF(kf[0][1]), qf[0][1], sa);
            SB();
#define EX(S, i) S[i] = __builtin_amdgcn_exp2f(S[i])
#define STEP(SC, SN, B, t, X1, X2, X3, X4) do { \
            constexpr int tn = ((t) + 1) & 3, bn = (t) == 3 ? ((B) + 1) & 3 : (B), tp = ((t) + 3) & 3, bp = (t) == 0 ? ((B) + 3) & 3 : (B); \
            o[tp] = MFMA32(BF(vf[bp][0]), BF(plo), o[tp]); SB(); X1; SB(); \
            phi[2] = cvt2(SN[12], SN[13]); phi[3] = cvt2(SN[14], SN[15]); PIN(phi); SB(); EX(SC, 0); EX(SC, 1); EX(SC, 2); EX(SC, 3); PIN(SC); SB(); \
            SN = MFMA32(BF(kf[bn][0]), qf[tn][0], zero); SB(); X2; SB(); \
            plo[0] = cvt2(SC[0], SC[1]); plo[1] = cvt2(SC[2], SC[3]); PIN(plo); SB(); EX(SC, 4); EX(SC, 5); EX(SC, 6); EX(SC, 7); PIN(SC); SB(); \
            SN = MFMA32(BF(kf[bn][1]), qf[tn][1], SN); SB(); X3; SB(); \
            plo[2] = cvt2(SC[4], SC[5]); plo[3] = cvt2(SC[6], SC[7]); PIN(plo); SB(); EX(SC, 8); EX(SC, 9); EX(SC, 10); EX(SC, 11); PIN(SC); SB(); \
            o[tp] = MFMA32(BF(vf[bp][1]), BF(phi), o[tp]); SB(); X4; SB(); \
            phi[0] = cvt2(SC[8], SC[9]); phi[1] = cvt2(SC[10], SC[11]); PIN(phi); SB(); EX(SC, 12); EX(SC, 13); EX(SC, 14); EX(SC, 15); PIN(SC); SB(); \
            } while (0)
#define NOP (void)0
#define PIN4(a, b, c_, d) asm volatile("" : "+v"(a), "+v"(b), "+v"(c_), "+v"(d))
#define PINS(x) asm volatile("" : "+s"(x))
#define BLOCK(B) \
            STEP(sa, sb, B, 0, vf[((B) + 3) & 3][0] = LDF(rv, 0, so), PIN4(vf[B][0], vf[B][1], kf[((B) + 1) & 3][0], kf[((B) + 1) & 3][1]), NOP, vf[((B) + 3) & 3][1] = LDF(rv, 1, so)); \
            STEP(sb, sa, B, 1, do { so = min(so + TREC, 15 * TREC); PINS(so); } while (0), NOP, NOP, NOP); \
            STEP(sa, sb, B, 2, NOP, kf[B][0] = LDF(rk, 0, so), kf[B][1] = LDF(rk, 1, so), NOP); \
            STEP(sb, sa, B, 3, NOP, NOP, NOP, NOP)
            STEP(sa, sb, 0, 0, vf[1][0] = LDF(rv, 0, TREC), vf[1][1] = LDF(rv, 1, TREC), kf[2][0] = LDF(rk, 0, 2 * TREC), kf[2][1] = LDF(rk, 1, 2 * TREC));
            STEP(sb, sa, 0, 1, vf[2][0] = LDF(rv, 0, 2 * TREC), vf[2][1] = LDF(rv, 1, 2 * TREC), kf[3][0] = LDF(rk, 0, 3 * TREC), kf[3][1] = LDF(rk, 1, 3 * TREC));
            STEP(sa, sb, 0, 2, vf[3][0] = LDF(rv, 0, 3 * TREC), vf[3][1] = LDF(rv, 1, 3 * TREC), kf[0][0] = LDF(rk, 0, 4 * TREC), kf[0][1] = LDF(rk, 1, 4 * TREC));
            STEP(sb, sa, 0, 3, do { if (lane == 0) __hip_atomic_fetch_add(misc + 8, 1u, RLX_WG); } while (0), NOP, NOP, NOP);
            unsigned so = 4 * TREC;
            for (int it = 0; it < 3; ++it) { BLOCK(1); BLOCK(2); BLOCK(3); BLOCK(0); }
            STEP(sa, sb, 1, 0, NOP, PIN4(vf[1][0], vf[1][1], kf[2][0], kf[2][1]), NOP, NOP); STEP(sb, sa, 1, 1, NOP, NOP, NOP, NOP); STEP(sa, sb, 1, 2, NOP, NOP, NOP, NOP); STEP(sb, sa, 1, 3, NOP, NOP, NOP, NOP);
            STEP(sa, sb, 2, 0, NOP, PIN4(vf[2][0], vf[2][1], kf[3][0], kf[3][1]), NOP, NOP); STEP(sb, sa, 2, 1, NOP, NOP, NOP, NOP); STEP(sa, sb, 2, 2, NOP, NOP, NOP, NOP); STEP(sb, sa, 2, 3, NOP, NOP, NOP, NOP);
            STEP(sa, sb, 3, 0, NOP, PIN4(vf[3][0], vf[3][1], kf[3][0], kf[3][1]), NOP, NOP); STEP(sb, sa, 3, 1, NOP, NOP, NOP, NOP);
            STEP(sa, sb, 3, 2, NOP, SENDQ(0, 0), SENDQ(0, 1), SENDQ(0, 2));
            STEP(sb, sa, 3, 3, SENDQ(0, 3), SENDQ(1, 0), SENDQ(1, 1), SENDQ(1, 2));
            o[3] = MFMA32(BF(vf[3][0]), BF(plo), o[3]); SB(); SENDQ(1, 3); SB();
            phi[2] = cvt2(sb[12], sb[13]); phi[3] = cvt2(sb[14], sb[15]); SB();
            SENDQ(2, 0); SB();
            o[3] = MFMA32(BF(vf[3][1]), BF(phi), o[3]); SB();
            SENDQ(2, 1); SENDQ(2, 2); SENDQ(2, 3);
        }
        PIN(fasti);
        if (__builtin_expect(__builtin_amdgcn_readfirstlane(fasti) == 0, 0)) {
            if (lane == 0) __hip_atomic_fetch_add(misc + 8, 1u, RLX_WG);
#pragma unroll
            for (int t = 0; t < 4; ++t) m[t] = -INFINITY;
            for (int j = 0; j < 16; ++j) {
                kf[0][0] = LDF(rk, 0, j * TREC); kf[0][1] = LDF(rk, 1, j * TREC); vf[0][0] = LDF(rv, 0, j * TREC); vf[0][1] = LDF(rv, 1, j * TREC);
#pragma unroll
                for (int t = 0; t < 4; ++t) {
                    SB();
                    f32x16 s = {};
                    s = MFMA32(BF(kf[0][0]), qf[t][0], s);
                    s = MFMA32(BF(kf[0][1]), qf[t][1], s);
                    float bm = s[0];
#pragma unroll
                    for (int i = 1; i < 16; ++i) bm = fmaxf(bm, s[i]);
                    bm = fmaxf(bm, swap32(bm, hh));
                    const float mn = fmaxf(m[t], bm), al = __builtin_amdgcn_exp2f(m[t] - mn);
                    m[t] = mn;
                    o[t] *= al; PIN(o[t]); SB();
#pragma unroll
                    for (int i = 0; i < 16; ++i) s[i] = __builtin_amdgcn_exp2f(s[i] - mn);
                    PIN(s); SB();
                    o[t] = MFMA32(BF(vf[0][0]), BF(pack_lo(s)), o[t]);
                    o[t] = MFMA32(BF(vf[0][1]), BF(pack_hi(s)), o[t]);
                    PIN(o[t]);
                }
            }
            SEND(0, m[0]); SEND(1, m[1]); SEND(2, m[2]);
            o[3][13] = hh ? m[3] : o[3][13];
        }
        f32x16 acc = o[3];
        BAR();
        f32x16 p[3];
#pragma unroll
        for (int d = 0; d < 3; ++d)
#pragma unroll
            for (int g = 0; g < 4; ++g) { const f32x4 v = *(LAS const f32x4*)(lds + cmb_slot(w4 * 3 + d) + g * 1024 + lane * 16);
#pragma unroll
                for (int i = 0; i < 4; ++i) p[d][4 * g + i] = v[i]; }
        const bool plain = __all(!hh || (acc[13] == 0.f && p[0][13] == 0.f && p[1][13] == 0.f && p[2][13] == 0.f));
        if (__builtin_expect(plain, 1)) {
            acc = (acc + p[0]) + (p[1] + p[2]);
        } else {
            float mm[4] = {acc[13], p[0][13], p[1][13], p[2][13]};
#pragma unroll
            for (int d = 0; d < 4; ++d) { const float s = swap32(mm[d], hh); mm[d] = hh ? mm[d] : s; }
            const float M = fmaxf(fmaxf(mm[0], mm[1]), fmaxf(mm[2], mm[3]));
            acc *= __builtin_amdgcn_exp2f(mm[0] - M);
#pragma unroll
            for (int d = 0; d < 3; ++d) acc += p[d] * __builtin_amdgcn_exp2f(mm[d + 1] - M);
        }
        const float lsw = swap32(acc[12], hh);
        const float inv = __builtin_amdgcn_rcpf(hh ? acc[12] : lsw);
        acc *= inv;
        u32x4 lo = pack_lo(acc), hi = pack_hi(acc);
        if (hh) { hi[2] = 0u; hi[3] = 0u; }
#pragma unroll
        for (int i = 0; i < 4; ++i) { lo[i] = min(lo[i], SENT - 1u); hi[i] = min(hi[i], SENT - 1u); }
        __builtin_amdgcn_raw_buffer_store_b128(lo, rws, ooff, 0, SC1);
        __builtin_amdgcn_raw_buffer_store_b128(hi, rws, ooff + 1024, 0, SC1);
        MEMFENCE();
        if (lane == 0) (void)__hip_atomic_exchange((GAS u64*)(ws + WS_OFLAG + (T * 16 + h) * 8), tag, RLX_AGENT);
        __builtin_amdgcn_endpgm();
    }
}

extern "C" void kernel_launch(void* const* d_in, const int* in_sizes, int n_in, void* d_out, int out_size, void* d_ws, size_t ws_size, hipStream_t stream) {
    (void)hipFuncSetAttribute((const void*)k_mha, hipFuncAttributeMaxDynamicSharedMemorySize, LDS_BYTES);
    k_mha<<<256, 512, LDS_BYTES, stream>>>((const __bf16*)d_in[0], (const __bf16*)d_in[1], (const __bf16*)d_in[2], (char*)d_ws, (__bf16*)d_out);
}
```
